```python
import torch

def _softmax(x, axis=-1):
    return torch.softmax(x, dim=axis)
def _gelu(x):
    return torch.nn.functional.gelu(x, approximate="tanh")
def _silu(x):
    return torch.nn.functional.silu(x)
def _sigmoid(x):
    return torch.sigmoid(x)
def _layernorm(x, axis=-1, eps=1e-5):
    m = x.mean(dim=axis, keepdim=True)
    v = x.var(dim=axis, keepdim=True, unbiased=False)
    return (x - m) / torch.sqrt(v + eps)
def _scatter_add(tgt, idx, src, axis=0):
    return tgt.clone().index_add_(axis, idx, src)
def _segment_sum(src, seg, n):
    out = torch.zeros((n,) + src.shape[1:], dtype=src.dtype)
    return out.index_add_(0, seg, src)
def _pad(x, pw):
    flat = tuple(v for pair in reversed(pw) for v in pair)
    return torch.nn.functional.pad(x, flat)

kernel_name = 'sequential_preLN__softmax_full_mha__dense_activation_relu__prompt'

def setup_inputs():

    _w = (lambda a: (lambda t: t.to(torch.bfloat16) if t.is_floating_point() else t)(torch.as_tensor(a)))
    def _A_setup():
        B, S, D = 1, 2048, 448
        return {
            "x": _w((lambda *s: torch.randn(*s, dtype=torch.bfloat16) * 0.05)(B, S, D)),
            "wqkv": _w((lambda *s: torch.randn(*s, dtype=torch.bfloat16) * 0.05)(D, 3 * D)),
            "wo": _w((lambda *s: torch.randn(*s, dtype=torch.bfloat16) * 0.05)(D, D)),
        }
    _A_in = _A_setup()

    def _B_setup():
        B, S, D, Dff = 1, 2048, 448, 1792
        return {
            "x": _w((lambda *s: torch.randn(*s, dtype=torch.bfloat16) * 0.05)(B, S, D)),
            "w1": _w((lambda *s: torch.randn(*s, dtype=torch.bfloat16) * 0.05)(D, Dff)),
            "w2": _w((lambda *s: torch.randn(*s, dtype=torch.bfloat16) * 0.05)(Dff, D)),
        }
    _B_in = _B_setup()

    return {
        "A_x": _A_in["x"],
        "A_wqkv": _A_in["wqkv"],
        "A_wo": _A_in["wo"],
        "B_x": _B_in["x"],
        "B_w1": _B_in["w1"],
        "B_w2": _B_in["w2"],
    }

def reference(A_x, A_wqkv, A_wo, B_x, B_w1, B_w2):
    def _A_ref(x, wqkv, wo):
        B, S, D = x.shape
        H = 16; Dh = D // H
        qkv = (lambda x, *d: x.permute(*d))((x @ wqkv).reshape(B, S, 3, H, Dh), 2, 0, 3, 1, 4)
        q, k, v = qkv[0], qkv[1], qkv[2]
        scale = Dh ** -0.5
        scores = torch.einsum("bhqd,bhkd->bhqk", q, k) * scale
        p = _softmax(scores, axis=-1)
        o = torch.einsum("bhqk,bhkd->bhqd", p, v)
        return (lambda x, *d: x.permute(*d))(o, 0, 2, 1, 3).reshape(B, S, D) @ wo

    def _B_ref(x, w1, w2):
        h = torch.einsum("bsd,df->bsf", x, w1)
        zero = torch.zeros_like(h)
        h = torch.maximum(h, zero)
        return torch.einsum("bsf,fd->bsd", h, w2)

    x = A_x
    h = x + _A_ref(_layernorm(x, axis=-1), A_wqkv, A_wo)
    h2 = h + _B_ref(_layernorm(h, axis=-1), B_w1, B_w2)
    return h2
```

```cpp
#include <hip/hip_runtime.h>
#include <hip/hip_bf16.h>
#include <stdint.h>

typedef __attribute__((ext_vector_type(8))) short bf16x8;
typedef __attribute__((ext_vector_type(4))) short s16x4;
typedef __attribute__((ext_vector_type(2))) __bf16 bf16x2;
typedef __attribute__((ext_vector_type(16))) float f32x16;
typedef __attribute__((ext_vector_type(4))) float f32x4;
typedef __attribute__((ext_vector_type(2))) float f32x2;
typedef __attribute__((ext_vector_type(4))) unsigned u32x4;
typedef __attribute__((ext_vector_type(2))) unsigned u32x2;
typedef __amdgpu_buffer_rsrc_t rsrc_t;
#define LAS __attribute__((address_space(3)))
typedef LAS char* lptr;

constexpr int S_ = 2048, D_ = 448, H_ = 16, DH = 28, DFF = 1792;
constexpr unsigned QKV_BYTES = 16u * 64u * 2048u;
constexpr unsigned OFF_Q = 0, OFF_K = QKV_BYTES, OFF_V = 2 * QKV_BYTES, OFF_O = 3 * QKV_BYTES;
constexpr unsigned O_BYTES = 64u * 32u * 1024u;
constexpr unsigned OFF_H = OFF_O + O_BYTES, H_BYTES = 64u * 28u * 1024u;
constexpr unsigned OFF_G = OFF_H + H_BYTES, G_BYTES = 64u * 112u * 1024u;
constexpr unsigned OOB = 0x7F000000u;
constexpr float QSCALE = 0.18898223650461362f * 1.4426950408889634f;
constexpr float EPS = 1e-5f;

#define MFMA(a, b, c) __builtin_amdgcn_mfma_f32_32x32x16_bf16((a), (b), (c), 0, 0, 0)
#define WAITV_BAR(N) asm volatile("s_waitcnt vmcnt(" #N ") lgkmcnt(0)\n\ts_barrier" ::: "memory")

__device__ __forceinline__ rsrc_t mkrs(const void* p, unsigned bytes) {
  return __builtin_amdgcn_make_buffer_rsrc((void*)p, (short)0, (int)bytes, 0x00020000);
}
__device__ __forceinline__ void dma16(rsrc_t r, unsigned voff, unsigned soff, unsigned ldsaddr) {
  asm volatile("s_mov_b32 m0, %0\n\ts_nop 0\n\tbuffer_load_dwordx4 %1, %2, %3 offen lds"
               :: "s"(ldsaddr), "v"(voff), "s"(r), "s"(soff) : "memory");
}
template <int IMM>
__device__ __forceinline__ void ld16(u32x4& d, rsrc_t r, unsigned voff, unsigned soff) {
  asm volatile("buffer_load_dwordx4 %0, %1, %2, %3 offen offset:%4" : "=v"(d) : "v"(voff), "s"(r), "s"(soff), "i"(IMM) : "memory");
}
__device__ __forceinline__ unsigned pk2(float a, float b) {
  f32x2 v = {a, b};
  return __builtin_bit_cast(unsigned, __builtin_convertvector(v, bf16x2));
}
__device__ __forceinline__ float bflo(unsigned u) { return __uint_as_float(u << 16); }
__device__ __forceinline__ float bfhi(unsigned u) { return __uint_as_float(u & 0xffff0000u); }
__device__ __forceinline__ float dot2(unsigned a, unsigned b, float c) {
  return __builtin_amdgcn_fdot2_f32_bf16(__builtin_bit_cast(bf16x2, a), __builtin_bit_cast(bf16x2, b), c, false);
}
__device__ __forceinline__ u32x4 lds128(lptr p) { return *(LAS u32x4*)p; }
__device__ __forceinline__ f32x4 ldsf4(lptr p) { return *(LAS f32x4*)p; }
__device__ __forceinline__ u32x2 ldstr(lptr p) {
  return __builtin_bit_cast(u32x2, __builtin_amdgcn_ds_read_tr16_b64_v4i16((LAS s16x4*)p));
}
__device__ __forceinline__ bf16x8 frag(u32x4 v) { return __builtin_bit_cast(bf16x8, v); }
__device__ __forceinline__ float xhalf_sum(float v) {
  auto r = __builtin_amdgcn_permlane32_swap(__float_as_uint(v), __float_as_uint(v), false, false);
  return __uint_as_float(r[0]) + __uint_as_float(r[1]);
}
__device__ __forceinline__ float xhalf_max(float v) {
  auto r = __builtin_amdgcn_permlane32_swap(__float_as_uint(v), __float_as_uint(v), false, false);
  return fmaxf(__uint_as_float(r[0]), __uint_as_float(r[1]));
}
constexpr unsigned ONES2 = 0x3F803F80u;

constexpr int K1_SLOT = 16384 + 12288, K1_LDS = 5 * K1_SLOT + 4 * 1024;

template <int NQ>
__device__ __forceinline__ void k1_body(lptr lds, rsrc_t rx, rsrc_t rw, char* ws, int rb, int cb, int w, int lane) {
  const int r = lane & 31, h = lane >> 5;
  const unsigned lbase = (unsigned)(uintptr_t)lds;
  unsigned vxa[2], vw[3];
#pragma unroll
  for (int p = 0; p < 2; ++p) vxa[p] = (unsigned)(lane >> 3) * 896u + (unsigned)((lane & 7) ^ ((4 * p + (lane >> 4)) & 7)) * 16u;
#pragma unroll
  for (int q = 0; q < 3; ++q) { const int i = q * 64 + lane; vw[q] = (unsigned)(16 * w + i / 12) * 2688u + (unsigned)(i % 12) * 16u; }
  const unsigned sx0 = (unsigned)(rb * 128 + w * 32) * 896u, sw0 = (unsigned)cb * 192u;
  auto issue = [&](int st, int slot) {
    const unsigned lb = lbase + slot * K1_SLOT;
    const unsigned sx = st < 7 ? sx0 + st * 128u : OOB, sw = st < 7 ? sw0 + st * (64u * 2688u) : OOB;
#pragma unroll
    for (int p = 0; p < 4; ++p) dma16(rx, vxa[p & 1], sx + p * 7168u, lb + (w * 4 + p) * 1024);
#pragma unroll
    for (int q = 0; q < 3; ++q) dma16(rw, vw[q], sw, lb + 16384 + (w * 3 + q) * 1024);
  };
#pragma unroll
  for (int st = 0; st < 4; ++st) issue(st, st);

  const int arow = w * 32 + r;
  const unsigned aoff = arow * 128 + ((h ^ ((arow >> 1) & 7)) * 16);
  const unsigned boff = 16384 + (8 * h + ((lane & 15) >> 2)) * 192 + ((lane >> 4) & 1) * 32 + (lane & 3) * 8;
  f32x16 acc[3] = {};
  float cs[3] = {0.f, 0.f, 0.f}, rs = 0.f, rq = 0.f;
  int slot = 0;
#pragma unroll 1
  for (int st = 0; st < 7; ++st) {
    WAITV_BAR(21);
    issue(st + 4, slot == 0 ? 4 : slot - 1);
    lptr sb = lds + slot * K1_SLOT;
#pragma unroll
    for (int ks = 0; ks < 4; ++ks) {
      const u32x4 a = lds128(sb + (aoff ^ (ks * 32)));
#pragma unroll
      for (int n = 0; n < 3; ++n) {
        const u32x2 b0 = ldstr(sb + boff + ks * 3072 + n * 64), b1 = ldstr(sb + boff + ks * 3072 + n * 64 + 768);
        const u32x4 b = {b0[0], b0[1], b1[0], b1[1]};
        acc[n] = n < NQ ? MFMA(frag(b), frag(a), acc[n]) : MFMA(frag(a), frag(b), acc[n]);
#pragma unroll
        for (int i = 0; i < 4; ++i) cs[n] = dot2(b[i], ONES2, cs[n]);
      }
#pragma unroll
      for (int i = 0; i < 4; ++i) { rs = dot2(a[i], ONES2, rs); rq = dot2(a[i], a[i], rq); }
    }
    slot = slot == 4 ? 0 : slot + 1;
  }
  asm volatile("s_waitcnt vmcnt(0)" ::: "memory");
  rs = xhalf_sum(rs); rq = xhalf_sum(rq);
  const float mu = rs * (1.f / 448.f);
  const float rstd = __builtin_amdgcn_rsqf(fmaxf(rq * (1.f / 448.f) - mu * mu, 0.f) + EPS);
  lptr sc = lds + 5 * K1_SLOT + w * 1024;
#pragma unroll
  for (int n = 0; n < 3; ++n) cs[n] = xhalf_sum(cs[n]);
  if (h == 0) {
#pragma unroll
    for (int n = 0; n < 3; ++n) *(LAS float*)(sc + n * 128 + r * 4) = cs[n];
    *(LAS float*)(sc + 384 + r * 4) = rstd;
    *(LAS float*)(sc + 512 + r * 4) = mu * rstd;
  }
  const int t = rb * 4 + w;
#pragma unroll
  for (int n = 0; n < 3; ++n) {
    if (n < NQ) {
#pragma unroll
      for (int g = 0; g < 4; ++g) {
        const f32x4 c4 = ldsf4(sc + n * 128 + (8 * g + 4 * h) * 4);
        const int c0 = cb * 96 + n * 32 + 8 * g + 4 * h;
        const bool isk = c0 >= 448;
        const int cc = isk ? c0 - 448 : c0, hd = cc / 28, d0 = cc - hd * 28;
        const float sc_ = isk ? rstd : rstd * QSCALE;
        float v[4];
#pragma unroll
        for (int i = 0; i < 4; ++i) v[i] = (acc[n][4 * g + i] - mu * c4[i]) * sc_;
        char* dst = ws + (isk ? OFF_K : OFF_Q) + (unsigned)((hd * 64 + t) * 2 + (d0 >> 4)) * 1024u + (((d0 >> 3) & 1) * 32 + r) * 16 + ((d0 >> 2) & 1) * 8;
        *(u32x2*)dst = (u32x2){pk2(v[0], v[1]), pk2(v[2], v[3])};
        if (d0 == 24) *(u32x2*)(dst + 8) = isk ? (u32x2){ONES2, 0x00003F80u} : (u32x2){0u, 0u};
      }
    } else {
      const int c = cb * 96 + n * 32 + r - 896, hd = c / 28, d = c - hd * 28;
      char* dst = ws + OFF_V + (unsigned)((hd * 64 + t) * 2) * 1024u + (h * 32 + d) * 16;
#pragma unroll
      for (int s = 0; s < 2; ++s) {
        u32x4 o;
#pragma unroll
        for (int gg = 0; gg < 2; ++gg) {
          const int g = 2 * s + gg;
          const f32x4 r4 = ldsf4(sc + 384 + (8 * g + 4 * h) * 4), m4 = ldsf4(sc + 512 + (8 * g + 4 * h) * 4);
          float v[4];
#pragma unroll
          for (int i = 0; i < 4; ++i) v[i] = acc[n][4 * g + i] * r4[i] - m4[i] * cs[n];
          o[2 * gg] = pk2(v[0], v[1]); o[2 * gg + 1] = pk2(v[2], v[3]);
        }
        *(u32x4*)(dst + s * 1024) = o;
        if (d == 27) *(u32x4*)(dst + s * 1024 + 16) = (u32x4){ONES2, ONES2, ONES2, ONES2};
      }
    }
  }
}

__global__ __launch_bounds__(256, 1) void k1_qkv(const char* __restrict__ x, const char* __restrict__ wqkv, char* __restrict__ ws) {
  extern __shared__ __attribute__((aligned(16))) char smem[];
  const int lane = threadIdx.x & 63, w = __builtin_amdgcn_readfirstlane(threadIdx.x >> 6);
  const int xcd = blockIdx.x & 7, j = blockIdx.x >> 3;
  const int rb = (xcd >> 1) * 4 + (j & 3), cb = (xcd & 1) * 7 + (j >> 2);
  const rsrc_t rx = mkrs(x, S_ * D_ * 2), rw = mkrs(wqkv, D_ * 3 * D_ * 2);
  if (cb < 9) k1_body<3>((lptr)smem, rx, rw, ws, rb, cb, w, lane);
  else if (cb == 9) k1_body<1>((lptr)smem, rx, rw, ws, rb, cb, w, lane);
  else k1_body<0>((lptr)smem, rx, rw, ws, rb, cb, w, lane);
}

constexpr int K2_LDS = 8 * 6 * 1024 + 8 * 2 * 256 + 64;

__device__ __forceinline__ void attn_tile(const u32x4& k0, const u32x4& k1, const u32x4& v0, const u32x4& v1,
                                          const u32x4 (&q)[2][2], f32x16 (&o)[2]) {
  const f32x16 z = {};
#pragma unroll
  for (int b = 0; b < 2; ++b) {
    f32x16 s = MFMA(frag(k0), frag(q[b][0]), z);
    s = MFMA(frag(k1), frag(q[b][1]), s);
    u32x4 p0, p1;
#pragma unroll
    for (int i = 0; i < 4; ++i) {
      p0[i] = pk2(__builtin_amdgcn_exp2f(s[2 * i]), __builtin_amdgcn_exp2f(s[2 * i + 1]));
      p1[i] = pk2(__builtin_amdgcn_exp2f(s[8 + 2 * i]), __builtin_amdgcn_exp2f(s[8 + 2 * i + 1]));
    }
    o[b] = MFMA(frag(v0), frag(p0), o[b]);
    o[b] = MFMA(frag(v1), frag(p1), o[b]);
  }
}

__global__ __launch_bounds__(512, 2) void k2_attn(char* __restrict__ ws) {
  extern __shared__ __attribute__((aligned(16))) char smem[];
  lptr lds = (lptr)smem;
  const int lane = threadIdx.x & 63, w = __builtin_amdgcn_readfirstlane(threadIdx.x >> 6);
  const int r = lane & 31, h = lane >> 5;
  const int hd = (blockIdx.x & 7) * 2 + ((blockIdx.x >> 3) & 1), rb = blockIdx.x >> 4;
  const int qp = w & 1, kq = w >> 1;
  const rsrc_t rq = mkrs(ws + OFF_Q, QKV_BYTES), rk = mkrs(ws + OFF_K, QKV_BYTES), rv = mkrs(ws + OFF_V, QKV_BYTES);
  const unsigned vo = lane * 16;
  const unsigned sq = (unsigned)(hd * 64 + rb * 4 + qp * 2) * 2048u, skv = (unsigned)(hd * 64 + kq * 16) * 2048u;
  u32x4 q[2][2], ka0, ka1, va0, va1, kb0, kb1, vb0, vb1;
  ld16<0>(q[0][0], rq, vo, sq); ld16<1024>(q[0][1], rq, vo, sq); ld16<2048>(q[1][0], rq, vo, sq); ld16<3072>(q[1][1], rq, vo, sq);
  LAS unsigned* flag = (LAS unsigned*)(lds + 8 * 6 * 1024 + 8 * 2 * 256);
  if (threadIdx.x == 0) *flag = 0;
  f32x16 o[2];
  float cf[2];
#pragma unroll 1
  for (int attempt = 0; attempt < 2; ++attempt) {
    float m[2] = {-INFINITY, -INFINITY};
    const int nmax = attempt ? 16 : 1;
    asm volatile("s_waitcnt vmcnt(0)" : "+v"(q[0][0]), "+v"(q[0][1]), "+v"(q[1][0]), "+v"(q[1][1]) :: "memory");
    if (h) { q[0][1][2] = 0; q[0][1][3] = 0; q[1][1][2] = 0; q[1][1][3] = 0; }
#pragma unroll 1
    for (int i = 0; i < nmax; ++i) {
      ld16<0>(ka0, rk, vo, skv + i * 2048u); ld16<1024>(ka1, rk, vo, skv + i * 2048u);
      asm volatile("s_waitcnt vmcnt(0)" : "+v"(ka0), "+v"(ka1) :: "memory");
      const f32x16 z = {};
#pragma unroll
      for (int b = 0; b < 2; ++b) {
        f32x16 s = MFMA(frag(ka0), frag(q[b][0]), z);
        s = MFMA(frag(ka1), frag(q[b][1]), s);
#pragma unroll
        for (int e = 0; e < 16; ++e) m[b] = fmaxf(m[b], s[e]);
      }
    }
    ld16<0>(ka0, rk, vo, skv); ld16<1024>(ka1, rk, vo, skv); ld16<0>(va0, rv, vo, skv); ld16<1024>(va1, rv, vo, skv);
    ld16<2048>(kb0, rk, vo, skv); ld16<3072>(kb1, rk, vo, skv); ld16<2048>(vb0, rv, vo, skv); ld16<3072>(vb1, rv, vo, skv);
#pragma unroll
    for (int b = 0; b < 2; ++b) {
      m[b] = xhalf_max(m[b]);
      const float nm = -m[b];
      const unsigned u0 = __float_as_uint(nm) & 0xffff0000u;
      const float r1 = nm - __uint_as_float(u0);
      const unsigned u1 = __float_as_uint(r1) & 0xffff0000u;
      const float r2 = r1 - __uint_as_float(u1);
      if (h) { q[b][1][2] = (u0 >> 16) | u1; q[b][1][3] = __float_as_uint(r2) >> 16; }
      o[b] = f32x16{};
    }
#pragma unroll 1
    for (int i = 0; i < 16; i += 2) {
      const unsigned sn = i < 14 ? skv + (i + 2) * 2048u : OOB;
      asm volatile("s_waitcnt vmcnt(4)" : "+v"(ka0), "+v"(ka1), "+v"(va0), "+v"(va1) :: "memory");
      attn_tile(ka0, ka1, va0, va1, q, o);
      ld16<0>(ka0, rk, vo, sn); ld16<1024>(ka1, rk, vo, sn); ld16<0>(va0, rv, vo, sn); ld16<1024>(va1, rv, vo, sn);
      asm volatile("s_waitcnt vmcnt(4)" : "+v"(kb0), "+v"(kb1), "+v"(vb0), "+v"(vb1) :: "memory");
      attn_tile(kb0, kb1, vb0, vb1, q, o);
      ld16<2048>(kb0, rk, vo, sn); ld16<3072>(kb1, rk, vo, sn); ld16<2048>(vb0, rv, vo, sn); ld16<3072>(vb1, rv, vo, sn);
    }
    asm volatile("s_waitcnt vmcnt(0)" : "+v"(ka0), "+v"(ka1), "+v"(va0), "+v"(va1), "+v"(kb0), "+v"(kb1), "+v"(vb0), "+v"(vb1) :: "memory");
    lptr ml = lds + 8 * 6 * 1024;
    bool bad = false;
#pragma unroll
    for (int b = 0; b < 2; ++b) {
      const float l = o[b][12];
      if (h) {
        *(LAS f32x2*)(ml + ((qp * 2 + b) * 32 + r) * 32 + kq * 8) = (f32x2){m[b], l};
        bad |= !(l >= 0x1p-30f && l <= 0x1p30f);
      }
    }
    if (bad) *flag = 1;
    __syncthreads();
    const unsigned redo = *flag;
    if (attempt == 0 && redo) { __syncthreads(); continue; }
#pragma unroll
    for (int b = 0; b < 2; ++b) {
      const f32x4 a0 = ldsf4(ml + ((qp * 2 + b) * 32 + r) * 32), a1 = ldsf4(ml + ((qp * 2 + b) * 32 + r) * 32 + 16);
      const float M = fmaxf(fmaxf(a0[0], a0[2]), fmaxf(a1[0], a1[2]));
      const float L = a0[1] * __builtin_amdgcn_exp2f(a0[0] - M) + a0[3] * __builtin_amdgcn_exp2f(a0[2] - M) +
                      a1[1] * __builtin_amdgcn_exp2f(a1[0] - M) + a1[3] * __builtin_amdgcn_exp2f(a1[2] - M);
      cf[b] = __builtin_amdgcn_exp2f(m[b] - M) / L;
    }
    break;
  }
  f32x4 own[2];
#pragma unroll
  for (int f = 0; f < 4; ++f) {
    const int b = f & 1, s = f >> 1;
    f32x4 lo, hi;
#pragma unroll
    for (int i = 0; i < 4; ++i) { lo[i] = o[b][8 * s + i] * cf[b]; hi[i] = o[b][8 * s + 4 + i] * cf[b]; }
    if (f == kq) { own[0] = lo; own[1] = hi; }
    else {
      const int slot = (kq < f ? kq : kq - 1);
      lptr dst = lds + ((qp * 4 + f) * 3 + slot) * 2048 + lane * 16;
      *(LAS f32x4*)dst = lo; *(LAS f32x4*)(dst + 1024) = hi;
    }
  }
  __syncthreads();
#pragma unroll
  for (int sl = 0; sl < 3; ++sl) {
    lptr src = lds + ((qp * 4 + kq) * 3 + sl) * 2048 + lane * 16;
    own[0] += ldsf4(src); own[1] += ldsf4(src + 1024);
  }
  u32x4 res = {pk2(own[0][0], own[0][1]), pk2(own[0][2], own[0][3]), pk2(own[1][0], own[1][1]), pk2(own[1][2], own[1][3])};
  if (h && (kq >> 1)) { res[2] = 0; res[3] = 0; }
  const int t = rb * 4 + qp * 2 + (kq & 1);
  *(u32x4*)(ws + OFF_O + (unsigned)((t * 16 + hd) * 2 + (kq >> 1)) * 1024u + vo) = res;
}

constexpr int K35_LDS = 4 * 32768;

template <bool FINAL>
__global__ __launch_bounds__(256, 1) void k35_proj(const char* __restrict__ x, const char* __restrict__ wmat, char* __restrict__ ws, char* __restrict__ out) {
  extern __shared__ __attribute__((aligned(16))) char smem[];
  lptr lds = (lptr)smem;
  constexpr int NST = FINAL ? 14 : 4, KSTEPS = FINAL ? 112 : 32, WROWS = FINAL ? 128 : 112;
  const int lane = threadIdx.x & 63, w = __builtin_amdgcn_readfirstlane(threadIdx.x >> 6);
  const int r = lane & 31, h = lane >> 5;
  const int xcd = blockIdx.x & 7, j = blockIdx.x >> 3;
  const int rb = xcd * 4 + (j & 3), cb = j >> 2;
  const int wr = w & 1, wc = w >> 1;
  const int t = rb * 2 + wr, sub = cb * 2 + wc;
  const rsrc_t ra = FINAL ? mkrs(ws + OFF_G, G_BYTES) : mkrs(ws + OFF_O, O_BYTES);
  const rsrc_t rw = mkrs(wmat, (FINAL ? DFF : D_) * D_ * 2);
  const unsigned lbase = (unsigned)(uintptr_t)lds;
  u32x4 res[2];
  if (FINAL) {
#pragma unroll
    for (int s = 0; s < 2; ++s) res[s] = *(const u32x4*)(ws + OFF_H + (unsigned)(t * 28 + sub * 2 + s) * 1024u + lane * 16);
  } else {
#pragma unroll
    for (int g = 0; g < 4; ++g) {
      const u32x2 v = *(const u32x2*)(x + (unsigned)(t * 32 + r) * 896u + (sub * 32 + 8 * g + 4 * h) * 2);
      res[g >> 1][(g & 1) * 2] = v[0]; res[g >> 1][(g & 1) * 2 + 1] = v[1];
    }
  }
  const unsigned va = lane * 16;
  const unsigned vw = (unsigned)(lane >> 3) * 896u + (unsigned)((lane & 7) ^ (((lane >> 4) & 1) << 2)) * 16u;
  const unsigned sa0 = (unsigned)((rb * 2 + (w >> 1)) * KSTEPS + (w & 1) * 4) * 1024u;
  const unsigned sw0 = (unsigned)(w * 32) * 896u + cb * 128u;
  auto issue = [&](int st, int slot) {
    const unsigned lb = lbase + slot * 32768;
    const unsigned sa = st < NST ? sa0 + st * 8192u : OOB, sw = st < NST ? sw0 + st * (WROWS * 896u) : OOB;
#pragma unroll
    for (int p = 0; p < 4; ++p) dma16(ra, va, sa + p * 1024u, lb + (w * 4 + p) * 1024);
#pragma unroll
    for (int p = 0; p < 4; ++p) dma16(rw, vw, sw + p * 7168u, lb + 16384 + (w * 4 + p) * 1024);
  };
#pragma unroll
  for (int st = 0; st < 3; ++st) issue(st, st);
  const int q = (lane & 15) >> 2;
  const unsigned aoff = wr * 8192 + lane * 16;
  const unsigned boff = 16384 + (4 * h + q) * 128 + ((wc ^ (q >> 1)) * 64) + ((lane >> 4) & 1) * 32 + (lane & 3) * 8;
  f32x16 acc = {};
#pragma unroll 1
  for (int st = 0; st < NST; ++st) {
    WAITV_BAR(16);
    issue(st + 3, (st + 3) & 3);
    lptr sb = lds + (st & 3) * 32768;
#pragma unroll
    for (int ks = 0; ks < 8; ++ks) {
      const u32x4 a = lds128(sb + aoff + ks * 1024);
      const int row0 = FINAL ? ks * 16 : (ks >> 1) * 28 + (ks & 1) * 16;
      const u32x2 b0 = ldstr(sb + boff + row0 * 128), b1 = ldstr(sb + boff + (row0 + 8) * 128);
      const u32x4 b = {b0[0], b0[1], b1[0], b1[1]};
      acc = MFMA(frag(b), frag(a), acc);
    }
  }
  asm volatile("s_waitcnt vmcnt(0)" ::: "memory");
  u32x4 o[2];
#pragma unroll
  for (int s = 0; s < 2; ++s)
#pragma unroll
    for (int i = 0; i < 4; ++i) o[s][i] = pk2(acc[8 * s + 2 * i] + bflo(res[s][i]), acc[8 * s + 2 * i + 1] + bfhi(res[s][i]));
  if (FINAL) {
#pragma unroll
    for (int g = 0; g < 4; ++g)
      *(u32x2*)(out + (unsigned)(t * 32 + r) * 896u + (sub * 32 + 8 * g + 4 * h) * 2) = (u32x2){o[g >> 1][(g & 1) * 2], o[g >> 1][(g & 1) * 2 + 1]};
  } else {
#pragma unroll
    for (int s = 0; s < 2; ++s) *(u32x4*)(ws + OFF_H + (unsigned)(t * 28 + sub * 2 + s) * 1024u + lane * 16) = o[s];
  }
}

constexpr int K4_LDS = 4 * 32768 + 512;

__global__ __launch_bounds__(256, 1) void k4_ffn1(const char* __restrict__ w1, char* __restrict__ ws) {
  extern __shared__ __attribute__((aligned(16))) char smem[];
  lptr lds = (lptr)smem;
  const int lane = threadIdx.x & 63, w = __builtin_amdgcn_readfirstlane(threadIdx.x >> 6);
  const int r = lane & 31, h = lane >> 5;
  const int xcd = blockIdx.x & 7, j = blockIdx.x >> 3;
  const int rb = xcd * 2 + (j & 1), cb = j >> 1;
  const int t = rb * 4 + w;
  const rsrc_t ra = mkrs(ws + OFF_H, H_BYTES), rw = mkrs(w1, D_ * DFF * 2);
  const unsigned lbase = (unsigned)(uintptr_t)lds;
  const unsigned va = lane * 16;
  const unsigned vw = (unsigned)(lane >> 4) * 3584u + (unsigned)((lane & 15) ^ ((lane >> 4) << 2)) * 16u;
  const unsigned sa0 = (unsigned)(t * 28) * 1024u, sw0 = (unsigned)(w * 16) * 3584u + cb * 256u;
  auto issue = [&](int st, int slot) {
    const unsigned lb = lbase + slot * 32768;
    const unsigned sa = st < 7 ? sa0 + st * 4096u : OOB, sw = st < 7 ? sw0 + st * (64u * 3584u) : OOB;
#pragma unroll
    for (int p = 0; p < 4; ++p) dma16(ra, va, sa + p * 1024u, lb + (w * 4 + p) * 1024);
#pragma unroll
    for (int p = 0; p < 4; ++p) dma16(rw, vw, sw + p * 14336u, lb + 16384 + (w * 4 + p) * 1024);
  };
#pragma unroll
  for (int st = 0; st < 3; ++st) issue(st, st);
  const int q = (lane & 15) >> 2;
  const unsigned aoff = w * 4096 + lane * 16;
  unsigned boff[4];
#pragma unroll
  for (int i = 0; i < 4; ++i) boff[i] = 16384 + (4 * h + q) * 256 + ((((w + i) & 3) ^ q) * 64) + ((lane >> 4) & 1) * 32 + (lane & 3) * 8;
  f32x16 acc[4] = {};
  float cs = 0.f, rs = 0.f, rq = 0.f;
#pragma unroll 1
  for (int st = 0; st < 7; ++st) {
    WAITV_BAR(16);
    issue(st + 3, (st + 3) & 3);
    lptr sb = lds + (st & 3) * 32768;
#pragma unroll
    for (int ks = 0; ks < 4; ++ks) {
      const u32x4 a = lds128(sb + aoff + ks * 1024);
#pragma unroll
      for (int i = 0; i < 4; ++i) {
        const u32x2 b0 = ldstr(sb + boff[i] + ks * 4096), b1 = ldstr(sb + boff[i] + ks * 4096 + 2048);
        const u32x4 b = {b0[0], b0[1], b1[0], b1[1]};
        acc[i] = MFMA(frag(b), frag(a), acc[i]);
        if (i == 0) {
#pragma unroll
          for (int e = 0; e < 4; ++e) cs = dot2(b[e], ONES2, cs);
        }
      }
#pragma unroll
      for (int e = 0; e < 4; ++e) { rs = dot2(a[e], ONES2, rs); rq = dot2(a[e], a[e], rq); }
    }
  }
  asm volatile("s_waitcnt vmcnt(0)" ::: "memory");
  rs = xhalf_sum(rs); rq = xhalf_sum(rq); cs = xhalf_sum(cs);
  const float mu = rs * (1.f / 448.f);
  const float rstd = __builtin_amdgcn_rsqf(fmaxf(rq * (1.f / 448.f) - mu * mu, 0.f) + EPS);
  lptr sc = lds + 4 * 32768;
  if (h == 0) *(LAS float*)(sc + w * 128 + r * 4) = cs;
  __syncthreads();
#pragma unroll
  for (int i = 0; i < 4; ++i) {
    const int n = (w + i) & 3;
#pragma unroll
    for (int s = 0; s < 2; ++s) {
      u32x4 o;
#pragma unroll
      for (int gg = 0; gg < 2; ++gg) {
        const int g = 2 * s + gg;
        const f32x4 c4 = ldsf4(sc + n * 128 + (8 * g + 4 * h) * 4);
        float v[4];
#pragma unroll
        for (int e = 0; e < 4; ++e) v[e] = fmaxf((acc[i][4 * g + e] - mu * c4[e]) * rstd, 0.f);
        o[2 * gg] = pk2(v[0], v[1]); o[2 * gg + 1] = pk2(v[2], v[3]);
      }
      *(u32x4*)(ws + OFF_G + (unsigned)(t * 112 + (cb * 4 + n) * 2 + s) * 1024u + lane * 16) = o;
    }
  }
}

extern "C" void kernel_launch(void* const* d_in, const int* in_sizes, int n_in, void* d_out, int out_size,
                              void* d_ws, size_t ws_size, hipStream_t stream) {
  const char* x = (const char*)d_in[0];
  const char* wqkv = (const char*)d_in[1];
  const char* wo = (const char*)d_in[2];
  const char* w1 = (const char*)d_in[4];
  const char* w2 = (const char*)d_in[5];
  char* ws = (char*)d_ws;
  char* out = (char*)d_out;
  (void)hipFuncSetAttribute((const void*)k1_qkv, hipFuncAttributeMaxDynamicSharedMemorySize, K1_LDS);
  (void)hipFuncSetAttribute((const void*)k35_proj<false>, hipFuncAttributeMaxDynamicSharedMemorySize, K35_LDS);
  (void)hipFuncSetAttribute((const void*)k35_proj<true>, hipFuncAttributeMaxDynamicSharedMemorySize, K35_LDS);
  (void)hipFuncSetAttribute((const void*)k4_ffn1, hipFuncAttributeMaxDynamicSharedMemorySize, K4_LDS);
  k1_qkv<<<224, 256, K1_LDS, stream>>>(x, wqkv, ws);
  k2_attn<<<256, 512, K2_LDS, stream>>>(ws);
  k35_proj<false><<<224, 256, K35_LDS, stream>>>(x, wo, ws, out);
  k4_ffn1<<<224, 256, K4_LDS, stream>>>(w1, ws);
  k35_proj<true><<<224, 256, K35_LDS, stream>>>(x, w2, ws, out);
}
```

```cpp
#include <hip/hip_runtime.h>
#include <hip/hip_bf16.h>
#include <stdint.h>

typedef __attribute__((ext_vector_type(8))) short bf16x8;
typedef __attribute__((ext_vector_type(4))) short s16x4;
typedef __attribute__((ext_vector_type(2))) short s16x2;
typedef __attribute__((ext_vector_type(2))) __bf16 bf16x2;
typedef __attribute__((ext_vector_type(16))) float f32x16;
typedef __attribute__((ext_vector_type(8))) float f32x8;
typedef __attribute__((ext_vector_type(4))) float f32x4;
typedef __attribute__((ext_vector_type(2))) float f32x2;
typedef __attribute__((ext_vector_type(4))) unsigned u32x4;
typedef __attribute__((ext_vector_type(2))) unsigned u32x2;
typedef __amdgpu_buffer_rsrc_t rsrc_t;
#define LAS __attribute__((address_space(3)))
typedef LAS char* lptr;

constexpr int S_ = 2048, D_ = 448, DFF = 1792;
constexpr unsigned QKV_BYTES = 16u * 64u * 2048u;
constexpr unsigned OFF_Q = 0, OFF_K = QKV_BYTES, OFF_V = 2 * QKV_BYTES, OFF_O = 3 * QKV_BYTES;
constexpr unsigned OFF_H = OFF_O + 64u * 32u * 1024u;
constexpr unsigned OFF_G = OFF_H + 64u * 28u * 1024u;
constexpr unsigned OFF_ST = OFF_G + 64u * 112u * 1024u;
constexpr unsigned OFF_FL = OFF_ST + 14u * 2048u * 8u;
constexpr unsigned OFF_EP = OFF_FL + 4u * 256u * 256u;
constexpr unsigned WS_USED = OFF_EP + 256u;
constexpr unsigned OOB = 0x7F000000u;
constexpr int SLOT = 16384, L_WO = 6 * SLOT, L_DUMMY = L_WO + 57 * 1024, L_SCR = L_DUMMY + 1024, LDS_BYTES = 163840;
constexpr int L_CNT = L_SCR, L_CS = L_SCR + 64, L_ROW = L_CS + 2048, L_ML = L_CS;
constexpr int P1_SLOT = 16384 + 12288;

constexpr float QSCALE = 0.18898223650461362f * 1.4426950408889634f;
constexpr float EPS = 1e-5f;
constexpr unsigned ONES2 = 0x3F803F80u;

#define MFMA(a, b, c) __builtin_amdgcn_mfma_f32_32x32x16_bf16((a), (b), (c), 0, 0, 0)
#define WAITV_BAR(N) asm volatile("s_waitcnt vmcnt(" #N ")\n\ts_barrier" ::: "memory")
#define BAR() asm volatile("s_waitcnt lgkmcnt(0)\n\ts_barrier" ::: "memory")
#define SBAR() __builtin_amdgcn_sched_barrier(0)
#define PIN(x) asm volatile("" : "+v"(x))
#define EX(v) __builtin_amdgcn_exp2f(v)
#define STAMP(kid, i) do {} while (0)

__device__ __forceinline__ rsrc_t mkrs(const void* p, unsigned bytes) {
  return __builtin_amdgcn_make_buffer_rsrc((void*)p, (short)0, (int)bytes, 0x00020000);
}
__device__ __forceinline__ void dma16(rsrc_t r, unsigned voff, unsigned soff, unsigned ldsaddr) {
  asm volatile("s_mov_b32 m0, %0\n\ts_nop 0\n\tbuffer_load_dwordx4 %1, %2, %3 offen lds"
               :: "s"(ldsaddr), "v"(voff), "s"(r), "s"(soff) : "memory");
}
__device__ __forceinline__ void dma16x4_sc1(rsrc_t r, unsigned voff, unsigned soff, unsigned ldsaddr) {
  asm volatile("s_mov_b32 m0, %0\n\ts_nop 0\n\tbuffer_load_dwordx4 %1, %2, %3 offen sc1 lds\n\t"
               "buffer_load_dwordx4 %1, %2, %3 offen offset:1024 sc1 lds\n\t"
               "buffer_load_dwordx4 %1, %2, %3 offen offset:2048 sc1 lds\n\t"
               "buffer_load_dwordx4 %1, %2, %3 offen offset:3072 sc1 lds"
               :: "s"(ldsaddr), "v"(voff), "s"(r), "s"(soff) : "memory");
}
template <int IMM>
__device__ __forceinline__ void ld16(u32x4& d, rsrc_t r, unsigned voff, unsigned soff) {
  asm volatile("buffer_load_dwordx4 %0, %1, %2, %3 offen offset:%4 sc1" : "=v"(d) : "v"(voff), "s"(r), "s"(soff), "i"(IMM) : "memory");
}
template <int IMM>
__device__ __forceinline__ void st16(u32x4 v, rsrc_t r, unsigned voff, unsigned soff) {
  asm volatile("buffer_store_dwordx4 %0, %1, %2, %3 offen offset:%4 sc1\n\ts_nop 1" :: "v"(v), "v"(voff), "s"(r), "s"(soff), "i"(IMM) : "memory");
}
__device__ __forceinline__ void st16s(int s, u32x4 v, rsrc_t r, unsigned voff, unsigned soff) {
  if (s) st16<1024>(v, r, voff, soff); else st16<0>(v, r, voff, soff);
}
__device__ __forceinline__ unsigned pk2(float a, float b) {
  f32x2 v = {a, b};
  return __builtin_bit_cast(unsigned, __builtin_convertvector(v, bf16x2));
}
__device__ __forceinline__ float bflo(unsigned u) { return __uint_as_float(u << 16); }
__device__ __forceinline__ float bfhi(unsigned u) { return __uint_as_float(u & 0xffff0000u); }
__device__ __forceinline__ float dot2(unsigned a, unsigned b, float c) {
  return __builtin_amdgcn_fdot2_f32_bf16(__builtin_bit_cast(bf16x2, a), __builtin_bit_cast(bf16x2, b), c, false);
}
__device__ __forceinline__ u32x4 lds128(lptr p) { return *(LAS u32x4*)p; }
__device__ __forceinline__ f32x4 ldsf4(lptr p) { return *(LAS f32x4*)p; }
__device__ __forceinline__ u32x2 ldstr(lptr p) {
  return __builtin_bit_cast(u32x2, __builtin_amdgcn_ds_read_tr16_b64_v4i16((LAS s16x4*)p));
}
__device__ __forceinline__ u32x4 ldstr2(lptr p, int d) {
  const u32x2 a = ldstr(p), b = ldstr(p + d);
  return (u32x4){a[0], a[1], b[0], b[1]};
}
__device__ __forceinline__ bf16x8 frag(u32x4 v) { return __builtin_bit_cast(bf16x8, v); }
__device__ __forceinline__ float xhalf_sum(float v) {
  auto r = __builtin_amdgcn_permlane32_swap(__float_as_uint(v), __float_as_uint(v), false, false);
  return __uint_as_float(r[0]) + __uint_as_float(r[1]);
}
__device__ __forceinline__ float xhalf_max(float v) {
  auto r = __builtin_amdgcn_permlane32_swap(__float_as_uint(v), __float_as_uint(v), false, false);
  return fmaxf(__uint_as_float(r[0]), __uint_as_float(r[1]));
}
__device__ __forceinline__ u32x4 colfrag(float cs, int h) {
  const unsigned a = pk2(cs, cs);
  const float lo = cs - bflo(a);
  const unsigned b = pk2(lo, lo);
  return (u32x4){h ? 0u : a, h ? 0u : b, 0u, 0u};
}
__device__ __forceinline__ u32x4 rowfrag(float nmu) {
  const unsigned a = pk2(nmu, nmu);
  const unsigned p = pk2(nmu, nmu - bflo(a));
  return (u32x4){p, p, 0u, 0u};
}

template <int V> struct IC { static constexpr int value = V; };
template <int I, int N, class F>
__device__ __forceinline__ void static_for(F&& f) {
  if constexpr (I < N) { f(IC<I>{}); static_for<I + 1, N>(f); }
}

struct Ctx {
  lptr lds; unsigned lbase;
  char* ws;
  rsrc_t rws;
  unsigned tag;
  int lane, w8, w, r, h;
};

template <int NW>
__device__ __forceinline__ void publish(const Ctx& c, int cnt_idx, unsigned flag_off) {
  asm volatile("s_waitcnt vmcnt(0)" ::: "memory");
  if (c.lane == 0) {
    const unsigned old = __hip_atomic_fetch_add((LAS unsigned*)(c.lds + L_CNT + cnt_idx * 4), 1u, __ATOMIC_RELAXED, __HIP_MEMORY_SCOPE_WORKGROUP);
    if (old == NW - 1) __builtin_amdgcn_raw_buffer_store_b32(c.tag, c.rws, 0, flag_off, 16);
  }
}
__device__ __forceinline__ void poll(const Ctx& c, unsigned voff) {
#pragma unroll 1
  for (int spins = 0; spins < (1 << 17); ++spins) {
    unsigned f;
    asm volatile("buffer_load_dword %0, %1, %2, 0 offen sc1\n\ts_waitcnt vmcnt(0)" : "=v"(f) : "v"(voff), "s"(c.rws) : "memory");
    if (__all(f == c.tag)) break;
  }
}
__device__ __forceinline__ unsigned flag_off(int phase, int idx) { return OFF_FL + (unsigned)(phase * 256 + idx) * 256u; }

template <int NQ>
__device__ __forceinline__ void p1_compute(const Ctx& c, int rb, int cb) {
  const int lane = c.lane, w = c.w, r = c.r, h = c.h;
  lptr lds = c.lds;
  const int arow = w * 32 + r;
  const unsigned aoff = arow * 128 + ((h ^ ((arow >> 1) & 7)) * 16);
  const unsigned boff = 16384 + (8 * h + ((lane & 15) >> 2)) * 192 + ((lane >> 4) & 1) * 32 + (lane & 3) * 8 + (cb & 1) * 8;
  unsigned ao[4], bo[4];
#pragma unroll
  for (int i = 0; i < 4; ++i) { const int ks = (w + i) & 3; ao[i] = aoff ^ (ks * 32); bo[i] = boff + ks * 3072; }
  f32x16 acc[3] = {};
  float cs[3] = {0.f, 0.f, 0.f}, rs[2] = {0.f, 0.f}, rq[2] = {0.f, 0.f};
  u32x4 a[4], b[4][3];
  auto dots = [&]() {
#pragma unroll
    for (int e = 0; e < 4; ++e) {
#pragma unroll
      for (int n = 0; n < 3; ++n) cs[n] = dot2(b[0][n][e], ONES2, cs[n]);
#pragma unroll
      for (int i = 0; i < 4; ++i) { rs[i & 1] = dot2(a[i][e], ONES2, rs[i & 1]); rq[i & 1] = dot2(a[i][e], a[i][e], rq[i & 1]); }
    }
    SBAR();
  };
  BAR();
  STAMP(0, 1);
#pragma unroll
  for (int i = 0; i < 4; ++i) {
    a[i] = lds128(lds + ao[i]);
#pragma unroll
    for (int n = 0; n < 3; ++n) b[i][n] = ldstr2(lds + bo[i] + n * 56, 768);
  }
  SBAR();
  int slot = 1;
#pragma unroll 1
  for (int st = 1; st < 7; ++st) {
    dots();
    BAR();
    lptr sb = lds + slot * P1_SLOT;
#pragma unroll
    for (int i = 0; i < 4; ++i)
#pragma unroll
      for (int n = 0; n < 3; ++n) {
        acc[n] = n < NQ ? MFMA(frag(b[i][n]), frag(a[i]), acc[n]) : MFMA(frag(a[i]), frag(b[i][n]), acc[n]);
        SBAR();
        b[i][n] = ldstr2(sb + bo[i] + n * 56, 768);
        if (n == 2) a[i] = lds128(sb + ao[i]);
        SBAR();
      }
    slot = slot == 2 ? 0 : slot + 1;
  }
  STAMP(0, 7);
  dots();
  const float mu = xhalf_sum(rs[0] + rs[1]) * (1.f / 448.f);
  const float rstd = __builtin_amdgcn_rsqf(fmaxf(xhalf_sum(rq[0] + rq[1]) * (1.f / 448.f) - mu * mu, 0.f) + EPS);
  lptr srow = lds + L_ROW + w * 128;
#pragma unroll
  for (int n = 0; n < 3; ++n) cs[n] = xhalf_sum(cs[n]);
  if (h == 0) {
#pragma unroll
    for (int n = 0; n < 3; ++n) *(LAS float*)(lds + L_CS + (n * 32 + r) * 16 + w * 4) = cs[n];
    if (NQ < 3) *(LAS float*)(srow + r * 4) = rstd;
  }
  BAR();
  const u32x4 rf = rowfrag(-mu);
  u32x4 cf[3];
  f32x4 p4[3], r4[4];
#pragma unroll
  for (int n = 0; n < 3; ++n) p4[n] = ldsf4(lds + L_CS + (n * 32 + r) * 16);
  if (NQ < 3) {
#pragma unroll
    for (int g = 0; g < 4; ++g) r4[g] = ldsf4(srow + (8 * g + 4 * h) * 4);
  }
  SBAR();
#pragma unroll
  for (int i = 0; i < 4; ++i)
#pragma unroll
    for (int n = 0; n < 3; ++n) {
      acc[n] = n < NQ ? MFMA(frag(b[i][n]), frag(a[i]), acc[n]) : MFMA(frag(a[i]), frag(b[i][n]), acc[n]);
      if (i == 2) {
        float t0 = p4[n][0] + p4[n][1], t1 = p4[n][2] + p4[n][3];
        PIN(t0); PIN(t1);
        cf[n] = colfrag(t0 + t1, h);
        PIN(cf[n]);
      }
      SBAR();
    }
#pragma unroll
  for (int n = 0; n < 3; ++n) acc[n] = n < NQ ? MFMA(frag(cf[n]), frag(rf), acc[n]) : MFMA(frag(rf), frag(cf[n]), acc[n]);
  STAMP(0, 8);
  const int t = rb * 4 + w;
#pragma unroll
  for (int n = 0; n < 3; ++n) {
    const int u = cb * 3 + n;
    const float sc = u < 16 ? rstd * QSCALE : rstd;
#pragma unroll
    for (int s = 0; s < 2; ++s) {
      u32x4 o;
#pragma unroll
      for (int i = 0; i < 4; ++i) {
        const int e = 8 * s + 2 * i;
        o[i] = n < NQ ? pk2(acc[n][e] * sc, acc[n][e + 1] * sc) : pk2(acc[n][e] * r4[e >> 2][e & 3], acc[n][e + 1] * r4[e >> 2][(e & 3) + 1]);
      }
      if (n < NQ) {
        if (s == 1 && h) { o[2] = u < 16 ? 0u : ONES2; o[3] = u < 16 ? 0u : 0x00003F80u; }
      } else if (r >= 28) {
        o = (u32x4){ONES2, ONES2, ONES2, ONES2};
      }
      st16s(s, o, c.rws, lane * 16, (unsigned)(u * 64 + t) * 2048u);
    }
  }
  STAMP(0, 9);
  publish<4>(c, 0, flag_off(0, cb * 16 + rb));
  STAMP(0, 10);
}

__device__ __forceinline__ void p1_load(const Ctx& c, const char* x, const char* wqkv, int rb, int cb) {
  const int lane = c.lane, w = c.w;
  const rsrc_t rx = mkrs(x, S_ * D_ * 2), rw = mkrs(wqkv, D_ * 3 * D_ * 2);
  unsigned vxa[2], vw[3];
#pragma unroll
  for (int p = 0; p < 2; ++p) vxa[p] = (unsigned)(lane >> 3) * 896u + (unsigned)((lane & 7) ^ ((4 * p + (lane >> 4)) & 7)) * 16u;
#pragma unroll
  for (int q = 0; q < 3; ++q) { const int i = q * 64 + lane; vw[q] = (unsigned)(i / 12) * 2688u + (unsigned)(i % 12) * 16u; }
  const unsigned sx0 = (unsigned)(rb * 128 + w * 32) * 896u, sw0 = (((unsigned)cb * 168u) & ~15u) + (unsigned)w * (16u * 2688u);
  int ks = (cb + 2 * rb) % 7, slot = 0;
  auto issue = [&]() {
    const unsigned lb = c.lbase + slot * P1_SLOT;
#pragma unroll
    for (int p = 0; p < 4; ++p) dma16(rx, vxa[p & 1], sx0 + ks * 128u + p * 7168u, lb + (w * 4 + p) * 1024);
#pragma unroll
    for (int q = 0; q < 3; ++q) dma16(rw, vw[q], sw0 + ks * (64u * 2688u), lb + 16384 + (w * 3 + q) * 1024);
    ks = ks == 6 ? 0 : ks + 1;
    slot = slot == 2 ? 0 : slot + 1;
  };
#pragma unroll 1
  for (int st = 0; st < 7; ++st) {
    if (st >= 2) WAITV_BAR(7);
    issue();
  }
  WAITV_BAR(7);
  WAITV_BAR(0);
  BAR();
}

#define ATT_UNIT(SC, SN, KN0, KN1, QN, OPREV, VPREV1, OCUR, VCUR0, HOOK_B, HOOK_C, HOOK_D)               \
  do {                                                                                                     \
    SN = MFMA(frag(KN0), frag(QN[0]), zero); SBAR();                                                       \
    p1[2] = pk2(e[4], e[5]); p1[3] = pk2(e[6], e[7]); PIN(p1); SBAR();                                     \
    e[0] = EX(SC[0]); e[1] = EX(SC[1]); e[2] = EX(SC[2]); e[3] = EX(SC[3]);                                \
    PIN(e); SBAR();                                                                                        \
    OPREV = MFMA(frag(VPREV1), frag(p1), OPREV); SBAR();                                                   \
    p0[0] = pk2(e[0], e[1]); p0[1] = pk2(e[2], e[3]); PIN(p0); SBAR();                                     \
    e[4] = EX(SC[4]); e[5] = EX(SC[5]); e[6] = EX(SC[6]); e[7] = EX(SC[7]);                                \
    PIN(e); SBAR(); HOOK_B;                                                                                \
    SN = MFMA(frag(KN1), frag(QN[1]), SN); SBAR();                                                         \
    p0[2] = pk2(e[4], e[5]); p0[3] = pk2(e[6], e[7]); PIN(p0); SBAR();                                     \
    e[0] = EX(SC[8]); e[1] = EX(SC[9]); e[2] = EX(SC[10]); e[3] = EX(SC[11]);                              \
    PIN(e); SBAR(); HOOK_C;                                                                                \
    OCUR = MFMA(frag(VCUR0), frag(p0), OCUR); SBAR();                                                      \
    p1[0] = pk2(e[0], e[1]); p1[1] = pk2(e[2], e[3]); PIN(p1); SBAR();                                     \
    e[4] = EX(SC[12]); e[5] = EX(SC[13]); e[6] = EX(SC[14]); e[7] = EX(SC[15]);                            \
    PIN(e); SBAR(); HOOK_D;                                                                                \
  } while (0)

__device__ __forceinline__ void p2_attn(const Ctx& c, const char* wo, const char* w1, const char* w2, int cb3, int cb4) {
  const int lane = c.lane, w = c.w8, r = c.r, h = c.h;
  lptr lds = c.lds;
  const int hd = (blockIdx.x & 7) * 2 + ((blockIdx.x >> 3) & 1), rb = blockIdx.x >> 4, j = blockIdx.x >> 3;
  const int qp = w & 1, kq = w >> 1;
  const unsigned vo = lane * 16;
  const unsigned sq = OFF_Q + (unsigned)(hd * 64 + rb * 4 + qp * 2) * 2048u;
  const unsigned sk = OFF_K + (unsigned)(hd * 64 + kq * 16) * 2048u, sv = sk + QKV_BYTES;
  const unsigned rot = (unsigned)(rb & 7) * 4096u;
  const rsrc_t rs = c.rws;
  const rsrc_t rt = mkrs(w < 2 ? w1 : w2, w < 4 ? D_ * DFF * 2 : 0);
  const unsigned vt = lane * 64;
  auto touch = [&](unsigned so) {
    asm volatile("s_mov_b32 m0, %0\n\ts_nop 0\n\tbuffer_load_dword %1, %2, %3 offen lds" :: "s"(c.lbase + L_DUMMY), "v"(vt), "s"(rt), "s"(so) : "memory");
  };
  if (w == 0) {
    const int u = lane < 16 ? 16 + hd : (lane < 32 ? 32 + hd : hd);
    poll(c, flag_off(0, (u / 3) * 16 + (lane < 32 ? (lane & 15) : rb)));
  }
  BAR();
  STAMP(1, w ? 16 : 0);
  LAS unsigned* flag = (LAS unsigned*)(lds + L_CNT + 32);
  const f32x16 zero = {};
  u32x4 ka0, ka1, va0, va1, kb0, kb1, vb0;
#pragma unroll 1
  for (int attempt = 0; attempt < 2; ++attempt) {
    u32x4 qa[2], qb[2], vb1;
    float m[2] = {-INFINITY, -INFINITY};
#pragma unroll 1
    for (int i = attempt ? 15 : 0; i >= 0; --i) {
      const unsigned ko = sk + ((rot + i * 2048u) & 32767u);
      ld16<0>(qa[0], rs, vo, sq); ld16<0>(ka0, rs, vo, ko); ld16<2048>(qb[0], rs, vo, sq);
      ld16<1024>(qa[1], rs, vo, sq); ld16<1024>(ka1, rs, vo, ko); ld16<3072>(qb[1], rs, vo, sq);
      ld16<0>(va0, rs, vo, sv + rot); ld16<1024>(va1, rs, vo, sv + rot);
      ld16<2048>(kb0, rs, vo, sk + rot); ld16<3072>(kb1, rs, vo, sk + rot);
      ld16<2048>(vb0, rs, vo, sv + rot);
      asm volatile("s_waitcnt vmcnt(5)" : "+v"(qa[0]), "+v"(qa[1]), "+v"(qb[0]), "+v"(qb[1]), "+v"(ka0), "+v"(ka1) :: "memory");
      f32x16 s0 = MFMA(frag(ka0), frag(qa[0]), zero), s1 = MFMA(frag(ka0), frag(qb[0]), zero);
      s0 = MFMA(frag(ka1), frag(qa[1]), s0); s1 = MFMA(frag(ka1), frag(qb[1]), s1);
#pragma unroll
      for (int e = 0; e < 16; ++e) { m[0] = fmaxf(m[0], s0[e]); m[1] = fmaxf(m[1], s1[e]); }
    }
#pragma unroll
    for (int b = 0; b < 2; ++b) {
      m[b] = xhalf_max(m[b]);
      const float nm = -m[b];
      const unsigned u0 = __float_as_uint(nm) & 0xffff0000u;
      const float r1 = nm - __uint_as_float(u0);
      const unsigned u1 = __float_as_uint(r1) & 0xffff0000u;
      const float r2 = r1 - __uint_as_float(u1);
      u32x4& qq = b ? qb[1] : qa[1];
      qq[2] = h ? ((u0 >> 16) | u1) : qq[2];
      qq[3] = h ? (__float_as_uint(r2) >> 16) : qq[3];
    }
    STAMP(1, w ? 17 : 1);
    f32x16 o0 = zero, o1 = zero;
    f32x16 sa = MFMA(frag(ka0), frag(qa[0]), zero), sb;
    sa = MFMA(frag(ka1), frag(qa[1]), sa);
    u32x4 p0 = {}, p1 = {};
    f32x8 e = {};
    vb1 = (u32x4){};
    touch(OOB);
    unsigned oc = rot, to = (unsigned)(j * 2 + (w & 1)) * 25088u;
#pragma unroll 1
    for (int i = 0; i < 8; ++i) {
      const unsigned on = (oc + 4096u) & 32767u;
      const unsigned kn = i < 7 ? sk + on : OOB, vn = i < 7 ? sv + on : OOB;
      ATT_UNIT(sa, sb, ka0, ka1, qb, o1, vb1, o0, va0,
               ld16<3072>(vb1, rs, vo, sv + oc),
               { ld16<0>(ka0, rs, vo, kn); ld16<1024>(ka1, rs, vo, kn);
                 asm volatile("s_waitcnt vmcnt(7)" : "+v"(va0), "+v"(va1) :: "memory"); },
               asm volatile("s_waitcnt vmcnt(5)" : "+v"(kb0), "+v"(kb1) :: "memory"));
      ATT_UNIT(sb, sa, kb0, kb1, qa, o0, va1, o1, va0,
               (void)0, (void)0,
               ld16<0>(va0, rs, vo, vn));
      ATT_UNIT(sa, sb, kb0, kb1, qb, o1, va1, o0, vb0,
               ld16<1024>(va1, rs, vo, vn),
               { ld16<2048>(kb0, rs, vo, kn); ld16<3072>(kb1, rs, vo, kn);
                 asm volatile("s_waitcnt vmcnt(6)" : "+v"(vb0), "+v"(vb1) :: "memory"); },
               asm volatile("s_waitcnt vmcnt(4)" : "+v"(ka0), "+v"(ka1) :: "memory"));
      ATT_UNIT(sb, sa, ka0, ka1, qa, o0, vb1, o1, vb0,
               (void)0, (void)0,
               { ld16<2048>(vb0, rs, vo, vn); touch(to); });
      oc = on; to += 4096u;
    }
    p1[2] = pk2(e[4], e[5]); p1[3] = pk2(e[6], e[7]);
    o1 = MFMA(frag(vb1), frag(p1), o1);
    STAMP(1, w ? 18 : 2);
#pragma unroll
    for (int f = 0; f < 4; ++f) {
      const f32x16& o = (f >> 1) ? o1 : o0;
      const int s = f & 1;
      lptr dst = lds + ((qp * 4 + (f >> 1) + 2 * s) * 4 + kq) * 2048 + lane * 16;
      *(LAS f32x4*)dst = (f32x4){o[8 * s], o[8 * s + 1], o[8 * s + 2], o[8 * s + 3]};
      *(LAS f32x4*)(dst + 1024) = (f32x4){o[8 * s + 4], o[8 * s + 5], o[8 * s + 6], o[8 * s + 7]};
    }
    bool bad = false;
#pragma unroll
    for (int b = 0; b < 2; ++b) {
      const float l = b ? o1[12] : o0[12];
      if (h) {
        *(LAS f32x2*)(lds + L_ML + ((qp * 2 + b) * 32 + r) * 32 + kq * 8) = (f32x2){m[b], l};
        bad |= !(l >= 0x1p-30f && l <= 0x1p30f);
      }
    }
    if (bad) *flag = 1;
    if (w < 4 && j < 28) {
      const rsrc_t rwo = mkrs(wo, D_ * D_ * 2), rw1 = mkrs(w1, D_ * DFF * 2);
      const unsigned vwo = (unsigned)(lane >> 3) * 896u + (unsigned)((lane & 7) ^ (((lane >> 4) & 1) << 2)) * 16u;
      const unsigned vw1 = (unsigned)(lane >> 4) * 3584u + (unsigned)((lane & 15) ^ ((lane >> 4) << 2)) * 16u;
#pragma unroll 1
      for (int p = w; p < 57; p += 4) dma16(rwo, vwo, p * 7168u + cb3 * 128u, c.lbase + L_WO + p * 1024);
#pragma unroll 1
      for (int p = w; p < 32; p += 4) dma16(rw1, vw1, p * 14336u + cb4 * 256u, c.lbase + 4 * SLOT + p * 1024);
      asm volatile("s_waitcnt vmcnt(0)" ::: "memory");
      STAMP(1, 3);
    }
    BAR();
    const unsigned redo = *flag;
    if (attempt || !redo) break;
    BAR();
  }
  STAMP(1, w ? 19 : 4);
  lptr mlp = lds + L_ML + ((qp * 2 + (kq & 1)) * 32 + r) * 32;
  const f32x4 a0 = ldsf4(mlp), a1 = ldsf4(mlp + 16);
  f32x4 lo[4], hi[4];
#pragma unroll
  for (int k = 0; k < 4; ++k) {
    lptr src = lds + ((qp * 4 + kq) * 4 + k) * 2048 + lane * 16;
    lo[k] = ldsf4(src); hi[k] = ldsf4(src + 1024);
  }
  const float M = fmaxf(fmaxf(a0[0], a0[2]), fmaxf(a1[0], a1[2]));
  float wk[4] = {EX(a0[0] - M), EX(a0[2] - M), EX(a1[0] - M), EX(a1[2] - M)};
  const float inv = __builtin_amdgcn_rcpf((a0[1] * wk[0] + a0[3] * wk[1]) + (a1[1] * wk[2] + a1[3] * wk[3]));
#pragma unroll
  for (int k = 0; k < 4; ++k) wk[k] *= inv;
  const f32x4 fl = (lo[0] * wk[0] + lo[1] * wk[1]) + (lo[2] * wk[2] + lo[3] * wk[3]);
  const f32x4 fh = (hi[0] * wk[0] + hi[1] * wk[1]) + (hi[2] * wk[2] + hi[3] * wk[3]);
  u32x4 res = {pk2(fl[0], fl[1]), pk2(fl[2], fl[3]), pk2(fh[0], fh[1]), pk2(fh[2], fh[3])};
  if (h && (kq >> 1)) { res[2] = 0; res[3] = 0; }
  const int t = rb * 4 + qp * 2 + (kq & 1);
  asm volatile("s_waitcnt vmcnt(0)" : "+v"(ka0), "+v"(ka1), "+v"(va0), "+v"(va1), "+v"(kb0), "+v"(kb1), "+v"(vb0) :: "memory");
  st16<0>(res, rs, vo, OFF_O + (unsigned)((t * 16 + hd) * 2 + (kq >> 1)) * 1024u);
  STAMP(1, w ? 20 : 5);
}
__device__ __forceinline__ void p2_publish(const Ctx& c) {
  publish<8>(c, 1, flag_off(1, (blockIdx.x >> 4) * 16 + (blockIdx.x & 7) * 2 + ((blockIdx.x >> 3) & 1)));
}

__device__ __forceinline__ void stats_issue(const Ctx& c, int t, f32x2 (&v)[7]) {
  const unsigned vo = (c.h * 7 * 2048 + c.r) * 8;
#pragma unroll
  for (int i = 0; i < 7; ++i)
    asm volatile("buffer_load_dwordx2 %0, %1, %2, %3 offen sc1" : "=v"(v[i]) : "v"(vo), "s"(c.rws), "s"(OFF_ST + (unsigned)(i * 2048 + t * 32) * 8u) : "memory");
}
#define STATS_WAIT(N, v) asm volatile("s_waitcnt vmcnt(" #N ")" : "+v"(v[0]), "+v"(v[1]), "+v"(v[2]), "+v"(v[3]), "+v"(v[4]), "+v"(v[5]), "+v"(v[6]) :: "memory")
__device__ __forceinline__ void stats_finish(const f32x2 (&v)[7], float& mu, float& rstd) {
  const f32x2 t = ((v[0] + v[1]) + (v[2] + v[3])) + ((v[4] + v[5]) + v[6]);
  mu = xhalf_sum(t[0]) * (1.f / 448.f);
  rstd = __builtin_amdgcn_rsqf(fmaxf(xhalf_sum(t[1]) * (1.f / 448.f) - mu * mu, 0.f) + EPS);
}
__device__ __forceinline__ unsigned relu2(float a, float b) {
  return __builtin_bit_cast(unsigned, __builtin_elementwise_max(__builtin_bit_cast(s16x2, pk2(a, b)), (s16x2){0, 0}));
}
constexpr int w1_slot(int st) { return st < 2 ? 4 + st : (st == 6 ? 6 : st - 2); }

__device__ __forceinline__ void p345_compute(const Ctx& c, const u32x2 (&xr)[4], char* out, int rb3, int cb3, int rb4, int cb4) {
  const int lane = c.lane, w = c.w, r = c.r, h = c.h;
  lptr lds = c.lds;
  const rsrc_t rs = c.rws;
  const int wr = w & 1, wc = w >> 1, q = (lane & 15) >> 2;
  const int t3 = rb3 * 2 + wr, sub = cb3 * 2 + wc;
  const unsigned trl = ((lane >> 4) & 1) * 32 + (lane & 3) * 8;
  const unsigned aoff = wr * 8192 + lane * 16;
  const unsigned boff = (4 * h + q) * 128 + ((wc ^ (q >> 1)) * 64) + trl;
  u32x4 hres[2];
  {
    u32x4 b[32];
    unsigned bw = L_WO + boff;
    PIN(bw);
#pragma unroll
    for (int ks = 0; ks < 32; ++ks) b[ks] = ldstr2(lds + bw + ((ks >> 1) * 28 + (ks & 1) * 16) * 128, 1024);
    SBAR();
    p2_publish(c);
    STAMP(1, 6);
    if (w == 0) poll(c, flag_off(1, (rb3 >> 1) * 16 + (lane & 15)));
    BAR();
    STAMP(2, 0);
    f32x16 acc = {};
#pragma unroll
    for (int st = 0; st < 4; ++st) {
      BAR();
      u32x4 a[8];
#pragma unroll
      for (int ks = 0; ks < 8; ++ks) a[ks] = lds128(lds + st * SLOT + aoff + ks * 1024);
      SBAR();
#pragma unroll
      for (int ks = 0; ks < 8; ++ks) acc = MFMA(frag(b[st * 8 + ks]), frag(a[ks]), acc);
      SBAR();
    }
    STAMP(2, 8);
    float s = 0.f, sq = 0.f;
#pragma unroll
    for (int g = 0; g < 4; ++g)
#pragma unroll
      for (int i = 0; i < 2; ++i) {
        const float v0 = acc[4 * g + 2 * i] + bflo(xr[g][i]), v1 = acc[4 * g + 2 * i + 1] + bfhi(xr[g][i]);
        hres[g >> 1][(g & 1) * 2 + i] = pk2(v0, v1);
        s += v0 + v1; sq += v0 * v0; sq += v1 * v1;
      }
#pragma unroll
    for (int sx = 0; sx < 2; ++sx) st16s(sx, hres[sx], rs, lane * 16, OFF_H + (unsigned)(t3 * 28 + sub * 2) * 1024u);
    s = xhalf_sum(s); sq = xhalf_sum(sq);
    if (h == 0) __builtin_amdgcn_raw_buffer_store_b64((u32x2){__float_as_uint(s), __float_as_uint(sq)}, rs, r * 8, OFF_ST + (unsigned)(sub * 2048 + t3 * 32) * 8u, 16);
    BAR();
    STAMP(2, 9);
    publish<4>(c, 2, flag_off(2, rb3 * 8 + cb3));
    STAMP(2, 10);
  }
  {
    const int t4 = rb4 * 4 + w;
    unsigned bo[4][2];
#pragma unroll
    for (int i = 0; i < 4; ++i) {
      bo[i][0] = (4 * h + q) * 256 + ((i ^ q) * 64) + trl; bo[i][1] = bo[i][0] + 65536;
      PIN(bo[i][0]); PIN(bo[i][1]);
    }
    if (w == 0) { const int l = lane < 14 ? lane : 0; poll(c, flag_off(2, (rb4 * 2 + (l >= 7)) * 8 + (l >= 7 ? l - 7 : l))); }
    BAR();
    STAMP(3, 0);
    const unsigned sa = OFF_H + (unsigned)(t4 * 28) * 1024u, va = lane * 16;
    const unsigned sg = OFF_G + (unsigned)(t4 * 112 + cb4 * 8) * 1024u;
    u32x4 a[28], bq[4][2];
    f32x2 sv[7];
#define P4_LOAD(st) do { ld16<0>(a[4 * (st)], rs, va, sa + (st) * 4096u); ld16<1024>(a[4 * (st) + 1], rs, va, sa + (st) * 4096u); \
                         ld16<2048>(a[4 * (st) + 2], rs, va, sa + (st) * 4096u); ld16<3072>(a[4 * (st) + 3], rs, va, sa + (st) * 4096u); } while (0)
#define P4_WAIT(st, N) asm volatile("s_waitcnt vmcnt(" #N ")" : "+v"(a[4 * (st)]), "+v"(a[4 * (st) + 1]), "+v"(a[4 * (st) + 2]), "+v"(a[4 * (st) + 3]) :: "memory")
    auto bread = [&](auto kc) {
      constexpr int k = decltype(kc)::value, pass = k / 28, ks = k % 28, off = w1_slot(ks >> 2) * SLOT + (ks & 3) * 4096;
      bq[k & 3][0] = ldstr2(lds + bo[2 * pass][off >> 16] + (off & 65535), 2048);
      bq[k & 3][1] = ldstr2(lds + bo[2 * pass + 1][off >> 16] + (off & 65535), 2048);
    };
    P4_LOAD(0); P4_LOAD(1); P4_LOAD(2);
    bread(IC<0>{}); bread(IC<1>{}); bread(IC<2>{});
    f32x16 acc[4] = {};
    u32x4 og;
    static_for<0, 56>([&](auto kc) {
      constexpr int k = decltype(kc)::value, pass = k / 28, ks = k % 28;
      if constexpr (k == 0) { P4_WAIT(0, 8); P4_LOAD(3); }
      if constexpr (k == 4) { P4_WAIT(1, 8); P4_LOAD(4); }
      if constexpr (k == 8) { P4_WAIT(2, 8); P4_LOAD(5); }
      if constexpr (k == 12) { P4_WAIT(3, 8); P4_LOAD(6); }
      if constexpr (k == 16) { P4_WAIT(4, 8); stats_issue(c, t4, sv); }
      if constexpr (k == 20) P4_WAIT(5, 11);
      if constexpr (k == 24) P4_WAIT(6, 7);
      if constexpr (k == 28) {
        float mu, rstd;
        STATS_WAIT(0, sv);
        stats_finish(sv, mu, rstd);
        const u32x4 rf = rowfrag(-mu);
        BAR();
#pragma unroll
        for (int i = 0; i < 4; ++i) acc[i] = MFMA(frag(colfrag(*(LAS float*)(lds + L_CS + i * 128 + r * 4), h)), frag(rf), acc[i]);
        SBAR();
      }
      acc[2 * pass] = MFMA(frag(bq[k & 3][0]), frag(a[ks]), acc[2 * pass]);
      SBAR();
      if constexpr (k + 3 < 56) bread(IC<k + 3>{});
      SBAR();
      acc[2 * pass + 1] = MFMA(frag(bq[k & 3][1]), frag(a[ks]), acc[2 * pass + 1]);
      SBAR();
      if constexpr (k >= 30 && k < 46) {
        constexpr int e = k - 30;
        og[e & 3] = relu2(acc[e >> 3][(e & 7) * 2], acc[e >> 3][(e & 7) * 2 + 1]);
        PIN(og);
        if constexpr ((e & 3) == 3) st16<(e >> 2) * 1024>(og, rs, va, sg);
        SBAR();
      }
    });
    STAMP(3, 8);
#pragma unroll
    for (int f = 0; f < 4; ++f) {
      u32x4 o;
#pragma unroll
      for (int e = 0; e < 4; ++e) o[e] = relu2(acc[2 + (f >> 1)][8 * (f & 1) + 2 * e], acc[2 + (f >> 1)][8 * (f & 1) + 2 * e + 1]);
      if (f == 0) st16<0>(o, rs, va, sg + 4096u); else if (f == 1) st16<1024>(o, rs, va, sg + 4096u);
      else if (f == 2) st16<2048>(o, rs, va, sg + 4096u); else st16<3072>(o, rs, va, sg + 4096u);
    }
    BAR();
    STAMP(3, 9);
    publish<4>(c, 3, flag_off(3, rb4 * 16 + cb4));
    STAMP(3, 10);
  }
  {
    if (w == 0) poll(c, flag_off(3, (rb3 >> 1) * 16 + (lane < 14 ? lane : 0)));
    BAR();
    STAMP(4, 0);
    float mu, rstd;
    f32x2 sv[7];
    stats_issue(c, t3, sv);
    STATS_WAIT(0, sv);
    stats_finish(sv, mu, rstd);
    f32x16 acc = {};
    u32x4 a[8], b[8];
    BAR();
    STAMP(4, 1);
#pragma unroll
    for (int ks = 0; ks < 8; ++ks) {
      a[ks] = lds128(lds + aoff + ks * 1024);
      b[ks] = ldstr2(lds + 7 * SLOT + boff + ks * 2048, 1024);
    }
    SBAR();
    int ws_ = 8;
#pragma unroll 1
    for (int st = 1; st < 14; ++st) {
      lptr pa = lds + (st & 3) * SLOT + aoff, pb = lds + ws_ * SLOT + boff;
      BAR();
#pragma unroll
      for (int ks = 0; ks < 8; ++ks) {
        acc = MFMA(frag(b[ks]), frag(a[ks]), acc);
        SBAR();
        a[ks] = lds128(pa + ks * 1024);
        b[ks] = ldstr2(pb + ks * 2048, 1024);
        SBAR();
      }
      ws_ = ws_ == 8 ? 4 : ws_ + 1;
    }
    STAMP(4, 15);
#pragma unroll
    for (int ks = 0; ks < 8; ++ks) acc = MFMA(frag(b[ks]), frag(a[ks]), acc);
    STAMP(4, 16);
#pragma unroll
    for (int sx = 0; sx < 2; ++sx) {
      u32x4 o;
#pragma unroll
      for (int i = 0; i < 4; ++i) o[i] = pk2(acc[8 * sx + 2 * i] * rstd + bflo(hres[sx][i]), acc[8 * sx + 2 * i + 1] * rstd + bfhi(hres[sx][i]));
      auto x0 = __builtin_amdgcn_permlane32_swap(o[0], o[2], false, false);
      auto x1 = __builtin_amdgcn_permlane32_swap(o[1], o[3], false, false);
      *(u32x4*)(out + (unsigned)(t3 * 32 + r) * 896u + (sub * 32 + 16 * sx + 8 * h) * 2) = (u32x4){x0[0], x1[0], x0[1], x1[1]};
    }
    STAMP(4, 18);
  }
}

__device__ __forceinline__ void p345_load(const Ctx& c, const char* w1, const char* w2, int rb3, int cb3, int cb4) {
  const int lane = c.lane, w = c.w;
  const rsrc_t rs = c.rws;
  const rsrc_t rw1 = mkrs(w1, D_ * DFF * 2), rw2 = mkrs(w2, D_ * DFF * 2);
  const unsigned va = lane * 16;
  const unsigned vw1 = (unsigned)(lane >> 4) * 3584u + (unsigned)((lane & 15) ^ ((lane >> 4) << 2)) * 16u;
  const unsigned vw2 = (unsigned)(lane >> 3) * 896u + (unsigned)((lane & 7) ^ (((lane >> 4) & 1) << 2)) * 16u;
  const unsigned lw = c.lbase + w * 4096;
  p2_publish(c);
  BAR();
  {
    const unsigned sa = OFF_O + (unsigned)((rb3 * 2 + (w >> 1)) * 32 + (w & 1) * 4) * 1024u;
#pragma unroll
    for (int st = 0; st < 4; ++st) dma16x4_sc1(rs, va, sa + st * 8192u, lw + st * SLOT);
  }
  WAITV_BAR(12); WAITV_BAR(8); WAITV_BAR(4); WAITV_BAR(0);
  BAR();
#pragma unroll 1
  for (int st = 2; st < 7; ++st)
#pragma unroll
    for (int p = 0; p < 4; ++p)
      dma16(rw1, vw1, (unsigned)(st * 16 + w * 4 + p) * 14336u + cb4 * 256u, lw + (st == 6 ? 6 : st - 2) * SLOT + p * 1024);
  WAITV_BAR(0);
  {
    const int q = (lane & 15) >> 2;
    lptr bw = c.lds + (4 * c.h + q) * 256 + ((w ^ q) * 64) + ((lane >> 4) & 1) * 32 + (lane & 3) * 8;
    float cs[4] = {0.f, 0.f, 0.f, 0.f};
#pragma unroll 1
    for (int sl = 0; sl < 4; ++sl) {
      u32x2 b[14];
#pragma unroll
      for (int k = 0; k < 14; ++k) b[k] = ldstr(bw + sl * 28672 + k * 2048);
      SBAR();
#pragma unroll
      for (int k = 0; k < 14; ++k) { cs[(k & 1) * 2] = dot2(b[k][0], ONES2, cs[(k & 1) * 2]); cs[(k & 1) * 2 + 1] = dot2(b[k][1], ONES2, cs[(k & 1) * 2 + 1]); }
    }
    const float csum = xhalf_sum((cs[0] + cs[1]) + (cs[2] + cs[3]));
    if (c.h == 0) *(LAS float*)(c.lds + L_CS + w * 128 + c.r * 4) = csum;
  }
  auto w2_issue = [&](int st, int slot) {
#pragma unroll
    for (int p = 0; p < 4; ++p) dma16(rw2, vw2, (unsigned)(st * 16 + w * 4 + p) * 7168u + cb3 * 128u, lw + slot * SLOT + p * 1024);
  };
  BAR();
  w2_issue(0, 7); w2_issue(1, 8);
  BAR();
  w2_issue(2, 4); w2_issue(3, 5); w2_issue(4, 6);
  BAR();
  const unsigned sg = OFF_G + (unsigned)((rb3 * 2 + (w >> 1)) * 112 + (w & 1) * 4) * 1024u;
  auto a_issue = [&](int st) { dma16x4_sc1(rs, va, sg + st * 8192u, lw + (st & 3) * SLOT); };
  a_issue(0); a_issue(1); a_issue(2);
  WAITV_BAR(8); a_issue(3);
  WAITV_BAR(8); w2_issue(5, 7); a_issue(4);
  WAITV_BAR(12); w2_issue(6, 8); a_issue(5);
  int ws_ = 4;
#pragma unroll 1
  for (int it = 3; it < 10; ++it) {
    WAITV_BAR(16); w2_issue(it + 4, ws_); a_issue(it + 3);
    ws_ = ws_ == 8 ? 4 : ws_ + 1;
  }
  WAITV_BAR(16); a_issue(13);
  WAITV_BAR(12); WAITV_BAR(4); WAITV_BAR(0);
}

__global__ __launch_bounds__(512, 2) void block_fwd(const char* __restrict__ x, const char* __restrict__ wqkv, const char* __restrict__ wo,
                                                    const char* __restrict__ w1, const char* __restrict__ w2,
                                                    char* __restrict__ ws, char* __restrict__ out) {
  extern __shared__ __attribute__((aligned(16))) char smem[];
  asm volatile("" :: "s"(x), "s"(wqkv), "s"(wo), "s"(w1), "s"(w2), "s"(ws), "s"(out));
  Ctx c;
  c.lds = (lptr)smem; c.lbase = (unsigned)(uintptr_t)smem; c.ws = ws;
  c.rws = mkrs(ws, WS_USED);
  c.lane = threadIdx.x & 63; c.w8 = __builtin_amdgcn_readfirstlane(threadIdx.x >> 6); c.w = c.w8 & 3;
  c.r = c.lane & 31; c.h = c.lane >> 5;
  const int xcd = blockIdx.x & 7, j = blockIdx.x >> 3;
  const int rb1 = (xcd >> 1) * 4 + (j & 3), cb1 = (xcd & 1) * 8 + (j >> 2);
  const int rb3 = xcd * 4 + (j & 3), cb3 = j >> 2, rb4 = xcd * 2 + (j & 1), cb4 = j >> 1;
  c.tag = 0;
  if (c.w8 >= 4) {
    STAMP(0, 0);
    p1_load(c, x, wqkv, rb1, cb1);
  } else if (threadIdx.x < 16) {
    *(LAS unsigned*)(c.lds + L_CNT + threadIdx.x * 4) = 0;
  }
  c.tag = __builtin_amdgcn_readfirstlane(__builtin_amdgcn_raw_buffer_load_b32(c.rws, 0, OFF_EP, 16)) * 0x9E3779B1u + 0x7F4A7C15u;
  if (c.w8 < 4) {
    if (cb1 < 10) p1_compute<3>(c, rb1, cb1);
    else if (cb1 == 10) p1_compute<2>(c, rb1, cb1);
    else p1_compute<0>(c, rb1, cb1);
  }
  u32x2 xr[4];
  {
    const rsrc_t rx = mkrs(x, S_ * D_ * 2);
    const unsigned vx = (unsigned)c.r * 896u + c.h * 8, sx = (unsigned)(rb3 * 2 + (c.w & 1)) * (32u * 896u) + (cb3 * 2 + (c.w >> 1)) * 64u;
#pragma unroll
    for (int g = 0; g < 4; ++g) xr[g] = __builtin_amdgcn_raw_buffer_load_b64(rx, vx + sx + g * 16, 0, 0);
  }
  p2_attn(c, wo, w1, w2, cb3, cb4);
  if (j >= 28) {
    p2_publish(c);
    if (blockIdx.x == 255 && c.w8 == 0) {
#pragma unroll 1
      for (int i = 0; i < 4; ++i) poll(c, flag_off(1, i * 64 + c.lane));
      if (c.lane == 0) __builtin_amdgcn_raw_buffer_store_b32(c.tag, c.rws, 0, OFF_EP, 16);
    }
    return;
  }
  if (c.w8 >= 4) p345_load(c, w1, w2, rb3, cb3, cb4);
  else p345_compute(c, xr, out, rb3, cb3, rb4, cb4);
}

extern "C" void kernel_launch(void* const* d_in, const int* in_sizes, int n_in, void* d_out, int out_size,
                              void* d_ws, size_t ws_size, hipStream_t stream) {
  (void)hipFuncSetAttribute((const void*)block_fwd, hipFuncAttributeMaxDynamicSharedMemorySize, LDS_BYTES);
  block_fwd<<<256, 512, LDS_BYTES, stream>>>((const char*)d_in[0], (const char*)d_in[1], (const char*)d_in[2],
                                             (const char*)d_in[4], (const char*)d_in[5], (char*)d_ws, (char*)d_out);
}
```

```cpp
#include <hip/hip_runtime.h>
#include <hip/hip_bf16.h>
#include <stdint.h>

typedef __attribute__((ext_vector_type(8))) short bf16x8;
typedef __attribute__((ext_vector_type(4))) short s16x4;
typedef __attribute__((ext_vector_type(2))) short s16x2;
typedef __attribute__((ext_vector_type(2))) __bf16 bf16x2;
typedef __attribute__((ext_vector_type(16))) float f32x16;
typedef __attribute__((ext_vector_type(8))) float f32x8;
typedef __attribute__((ext_vector_type(4))) float f32x4;
typedef __attribute__((ext_vector_type(2))) float f32x2;
typedef __attribute__((ext_vector_type(4))) unsigned u32x4;
typedef __attribute__((ext_vector_type(2))) unsigned u32x2;
typedef __amdgpu_buffer_rsrc_t rsrc_t;
#define LAS __attribute__((address_space(3)))
typedef LAS char* lptr;

constexpr int S_ = 2048, D_ = 448, DFF = 1792;
constexpr unsigned QKV_BYTES = 16u * 64u * 2048u;
constexpr unsigned OFF_Q = 0, OFF_K = QKV_BYTES, OFF_V = 2 * QKV_BYTES, OFF_O = 3 * QKV_BYTES;
constexpr unsigned OFF_H = OFF_O + 64u * 32u * 1024u;
constexpr unsigned OFF_G = OFF_H + 64u * 28u * 1024u;
constexpr unsigned OFF_ST = OFF_G + 64u * 112u * 1024u;
constexpr unsigned OFF_FL = OFF_ST + 14u * 2048u * 8u;
constexpr unsigned OFF_EP = OFF_FL + 4u * 256u * 256u;
constexpr unsigned WS_USED = OFF_EP + 256u;
constexpr unsigned OOB = 0x7F000000u;
constexpr int SLOT = 16384, L_WO = 6 * SLOT, L_DUMMY = L_WO + 57 * 1024, L_SCR = L_DUMMY + 1024, LDS_BYTES = 163840;
constexpr int L_CS = L_SCR, L_ROW = L_CS + 2048, L_ML = L_SCR, L_CNT = LDS_BYTES - 1024;
constexpr int L_RED3 = 2 * SLOT, L_RED5 = 35 * 4096;
constexpr int P1_SLOT = 16384 + 12288;
enum { EV_PUB1, EV_PUB2, EV_PUB3, EV_PUB4, EV_REDO, EV_WO, EV_BPRE, EV_GO3, EV_GO4, EV_GO5, EV_CMB0, EV_CMB1, EV_W1, EV_CS,
       EV_W1A, EV_A3, EV_RED3, EV_R3DONE, EV_A4, EV_X4, EV_W2A, EV_SEG0, EV_SEG1, EV_SEG2, EV_W2B, EV_RED5, EV_COUNT };

constexpr unsigned CODE_BYTES = 42 * 1024;
constexpr float QSCALE = 0.18898223650461362f * 1.4426950408889634f;
constexpr float EPS = 1e-5f;
constexpr unsigned ONES2 = 0x3F803F80u;

#define MFMA(a, b, c) __builtin_amdgcn_mfma_f32_32x32x16_bf16((a), (b), (c), 0, 0, 0)
#define WAITV_BAR(N) asm volatile("s_waitcnt vmcnt(" #N ")\n\ts_barrier" ::: "memory")
#define BAR() asm volatile("s_waitcnt lgkmcnt(0)\n\ts_barrier" ::: "memory")
#define SBAR() __builtin_amdgcn_sched_barrier(0)
#define PIN(x) asm volatile("" : "+v"(x))
#define EX(v) __builtin_amdgcn_exp2f(v)
#define STAMP(kid, i) do {} while (0)

__device__ __forceinline__ rsrc_t mkrs(const void* p, unsigned bytes) {
  return __builtin_amdgcn_make_buffer_rsrc((void*)p, (short)0, (int)bytes, 0x00020000);
}
__device__ __forceinline__ void dma16(rsrc_t r, unsigned voff, unsigned soff, unsigned ldsaddr) {
  asm volatile("s_mov_b32 m0, %0\n\ts_nop 0\n\tbuffer_load_dwordx4 %1, %2, %3 offen lds"
               :: "s"(ldsaddr), "v"(voff), "s"(r), "s"(soff) : "memory");
}
__device__ __forceinline__ void dma16x4_sc1(rsrc_t r, unsigned voff, unsigned soff, unsigned ldsaddr) {
  asm volatile("s_mov_b32 m0, %0\n\ts_nop 0\n\tbuffer_load_dwordx4 %1, %2, %3 offen sc1 lds\n\t"
               "buffer_load_dwordx4 %1, %2, %3 offen offset:1024 sc1 lds\n\t"
               "buffer_load_dwordx4 %1, %2, %3 offen offset:2048 sc1 lds\n\t"
               "buffer_load_dwordx4 %1, %2, %3 offen offset:3072 sc1 lds"
               :: "s"(ldsaddr), "v"(voff), "s"(r), "s"(soff) : "memory");
}
template <int IMM>
__device__ __forceinline__ void ld16(u32x4& d, rsrc_t r, unsigned voff, unsigned soff) {
  asm volatile("buffer_load_dwordx4 %0, %1, %2, %3 offen offset:%4 sc1" : "=v"(d) : "v"(voff), "s"(r), "s"(soff), "i"(IMM) : "memory");
}
__device__ __forceinline__ void ld16p(u32x4& d, rsrc_t r, unsigned voff, unsigned soff) {
  asm volatile("buffer_load_dwordx4 %0, %1, %2, %3 offen" : "=v"(d) : "v"(voff), "s"(r), "s"(soff) : "memory");
}
template <int N>
__device__ __forceinline__ void waitv(u32x4& x) { asm volatile("s_waitcnt vmcnt(%1)" : "+v"(x) : "i"(N) : "memory"); }
template <int IMM>
__device__ __forceinline__ void st16(u32x4 v, rsrc_t r, unsigned voff, unsigned soff) {
  asm volatile("buffer_store_dwordx4 %0, %1, %2, %3 offen offset:%4 sc1\n\ts_nop 1" :: "v"(v), "v"(voff), "s"(r), "s"(soff), "i"(IMM) : "memory");
}
__device__ __forceinline__ void st16s(int s, u32x4 v, rsrc_t r, unsigned voff, unsigned soff) {
  if (s) st16<1024>(v, r, voff, soff); else st16<0>(v, r, voff, soff);
}
__device__ __forceinline__ unsigned pk2(float a, float b) {
  f32x2 v = {a, b};
  return __builtin_bit_cast(unsigned, __builtin_convertvector(v, bf16x2));
}
__device__ __forceinline__ float bflo(unsigned u) { return __uint_as_float(u << 16); }
__device__ __forceinline__ float bfhi(unsigned u) { return __uint_as_float(u & 0xffff0000u); }
__device__ __forceinline__ float dot2(unsigned a, unsigned b, float c) {
  return __builtin_amdgcn_fdot2_f32_bf16(__builtin_bit_cast(bf16x2, a), __builtin_bit_cast(bf16x2, b), c, false);
}
__device__ __forceinline__ u32x4 lds128(lptr p) { return *(LAS u32x4*)p; }
__device__ __forceinline__ f32x4 ldsf4(lptr p) { return *(LAS f32x4*)p; }
__device__ __forceinline__ u32x2 ldstr(lptr p) {
  return __builtin_bit_cast(u32x2, __builtin_amdgcn_ds_read_tr16_b64_v4i16((LAS s16x4*)p));
}
__device__ __forceinline__ u32x4 ldstr2(lptr p, int d) {
  const u32x2 a = ldstr(p), b = ldstr(p + d);
  return (u32x4){a[0], a[1], b[0], b[1]};
}
__device__ __forceinline__ bf16x8 frag(u32x4 v) { return __builtin_bit_cast(bf16x8, v); }
__device__ __forceinline__ float xhalf_sum(float v) {
  auto r = __builtin_amdgcn_permlane32_swap(__float_as_uint(v), __float_as_uint(v), false, false);
  return __uint_as_float(r[0]) + __uint_as_float(r[1]);
}
__device__ __forceinline__ float xhalf_max(float v) {
  auto r = __builtin_amdgcn_permlane32_swap(__float_as_uint(v), __float_as_uint(v), false, false);
  return fmaxf(__uint_as_float(r[0]), __uint_as_float(r[1]));
}
__device__ __forceinline__ u32x4 colfrag(float cs, int h) {
  const unsigned a = pk2(cs, cs);
  const float lo = cs - bflo(a);
  const unsigned b = pk2(lo, lo);
  return (u32x4){h ? 0u : a, h ? 0u : b, 0u, 0u};
}
__device__ __forceinline__ u32x4 rowfrag(float nmu) {
  const unsigned a = pk2(nmu, nmu);
  const unsigned p = pk2(nmu, nmu - bflo(a));
  return (u32x4){p, p, 0u, 0u};
}

__device__ __forceinline__ void pin16(f32x16& x) { PIN(x); }
__device__ __forceinline__ void pin4(u32x4& x) { PIN(x); }
__device__ __forceinline__ void pinf(float& x) { PIN(x); }
template <int V> struct IC { static constexpr int value = V; };
template <int I, int N, class F>
__device__ __forceinline__ void static_for(F&& f) {
  if constexpr (I < N) { f(IC<I>{}); static_for<I + 1, N>(f); }
}

struct Ctx {
  lptr lds; unsigned lbase;
  char* ws;
  rsrc_t rws;
  unsigned tag;
  int lane, w8, w, r, h;
};

__device__ __forceinline__ void stampf(const Ctx& c, int kid, int i) { STAMP(kid, i); }
__device__ __forceinline__ void ev_set(const Ctx& c, int idx, int word) {
  const unsigned addr = c.lbase + L_CNT + idx * 16 + word * 4, one = 1;
  asm volatile("s_mov_b64 exec, 1\n\tds_write_b32 %0, %1\n\ts_mov_b64 exec, -1" :: "v"(addr), "v"(one) : "memory");
}
template <int NWORDS, int SLEEP = 0>
__device__ __forceinline__ void ev_wait(const Ctx& c, int idx) {
  const unsigned addr = c.lbase + L_CNT + idx * 16;
#pragma unroll 1
  for (int spins = 0; spins < (1 << 24); ++spins) {
    u32x4 v;
    asm volatile("ds_read_b128 %0, %1\n\ts_waitcnt lgkmcnt(0)" : "=v"(v) : "v"(addr) : "memory");
    if (__builtin_amdgcn_readfirstlane(NWORDS == 4 ? (v[0] & v[1] & v[2] & v[3]) : v[0])) break;
    if (SLEEP) __builtin_amdgcn_s_sleep(SLEEP);
  }
}
template <int SLEEP = 0>
__device__ __forceinline__ void ev_wait_half(const Ctx& c, int idx) {
  const unsigned addr = c.lbase + L_CNT + idx * 16 + (c.w >> 1) * 8;
#pragma unroll 1
  for (int spins = 0; spins < (1 << 24); ++spins) {
    u32x2 v;
    asm volatile("ds_read_b64 %0, %1\n\ts_waitcnt lgkmcnt(0)" : "=v"(v) : "v"(addr) : "memory");
    if (__builtin_amdgcn_readfirstlane(v[0] & v[1])) break;
    if (SLEEP) __builtin_amdgcn_s_sleep(SLEEP);
  }
}
template <int NW>
__device__ __forceinline__ void publish(const Ctx& c, int idx, unsigned flag_off) {
  const unsigned addr = c.lbase + L_CNT + idx * 16, one = 1;
  unsigned old;
  asm volatile("s_waitcnt vmcnt(0)\n\ts_mov_b64 exec, 1\n\tds_add_rtn_u32 %0, %1, %2\n\ts_mov_b64 exec, -1\n\ts_waitcnt lgkmcnt(0)"
               : "=v"(old) : "v"(addr), "v"(one) : "memory");
  if (__builtin_amdgcn_readfirstlane(old) == NW - 1) {
    const unsigned tag = c.tag;
    asm volatile("s_mov_b64 exec, 1\n\tbuffer_store_dword %0, off, %1, %2 sc1\n\ts_mov_b64 exec, -1" :: "v"(tag), "s"(c.rws), "s"(flag_off) : "memory");
  }
}
__device__ __forceinline__ void poll(const Ctx& c, unsigned voff) {
#pragma unroll 1
  for (int spins = 0; spins < (1 << 17); ++spins) {
    unsigned f;
    asm volatile("buffer_load_dword %0, %1, %2, 0 offen sc1\n\ts_waitcnt vmcnt(0)" : "=v"(f) : "v"(voff), "s"(c.rws) : "memory");
    if (__all(f == c.tag)) break;
  }
}
__device__ __forceinline__ void acquire(const Ctx& c, unsigned voff, int ev) {
  if (c.w == 0) { poll(c, voff); ev_set(c, ev, 0); }
  else ev_wait<1>(c, ev);
}
__device__ __forceinline__ unsigned flag_off(int phase, int idx) { return OFF_FL + (unsigned)(phase * 256 + idx) * 256u; }
__device__ __forceinline__ void touch_lds(const Ctx& c, rsrc_t r, unsigned voff, unsigned soff) {
  asm volatile("s_mov_b32 m0, %0\n\ts_nop 0\n\tbuffer_load_dword %1, %2, %3 offen lds" :: "s"(c.lbase + L_DUMMY), "v"(voff), "s"(r), "s"(soff) : "memory");
}

template <int NQ>
__device__ __forceinline__ void p1_compute(const Ctx& c, int rb, int cb) {
  const int lane = c.lane, w = c.w, r = c.r, h = c.h;
  lptr lds = c.lds;
  const int arow = w * 32 + r;
  const unsigned aoff = arow * 128 + ((h ^ ((arow >> 1) & 7)) * 16);
  const unsigned boff = 16384 + (8 * h + ((lane & 15) >> 2)) * 192 + ((lane >> 4) & 1) * 32 + (lane & 3) * 8 + (cb & 1) * 8;
  unsigned ao[4], bo[4];
#pragma unroll
  for (int i = 0; i < 4; ++i) { const int ks = (w + i) & 3; ao[i] = aoff ^ (ks * 32); bo[i] = boff + ks * 3072; }
  f32x16 acc[3] = {};
  float cs[3] = {0.f, 0.f, 0.f}, rs[2] = {0.f, 0.f}, rq[2] = {0.f, 0.f};
  u32x4 a[2][4], b[4][3];
  auto dots = [&](const u32x4 (&av)[4]) {
#pragma unroll
    for (int e = 0; e < 4; ++e) {
#pragma unroll
      for (int n = 0; n < 3; ++n) cs[n] = dot2(b[0][n][e], ONES2, cs[n]);
#pragma unroll
      for (int i = 0; i < 4; ++i) { rs[i & 1] = dot2(av[i][e], ONES2, rs[i & 1]); rq[i & 1] = dot2(av[i][e], av[i][e], rq[i & 1]); }
    }
    PIN(cs[0]); PIN(cs[1]); PIN(cs[2]); PIN(rs[0]); PIN(rs[1]); PIN(rq[0]); PIN(rq[1]);
    SBAR();
  };
  auto stage = [&](const u32x4 (&ac)[4], u32x4 (&an)[4], int st) {
    lptr sb = lds + (st >= 5 ? st - 5 : st) * P1_SLOT;
    dots(ac);
    asm volatile("s_barrier" ::: "memory");
#pragma unroll
    for (int i = 0; i < 4; ++i)
#pragma unroll
      for (int n = 0; n < 3; ++n) {
        acc[n] = n < NQ ? MFMA(frag(b[i][n]), frag(ac[i]), acc[n]) : MFMA(frag(ac[i]), frag(b[i][n]), acc[n]);
        SBAR();
        b[i][n] = ldstr2(sb + bo[i] + n * 56, 768);
        if (i * 3 + n < 4) an[i * 3 + n] = lds128(sb + ao[i * 3 + n]);
        SBAR();
      }
  };
  BAR();
  STAMP(0, 1);
#pragma unroll
  for (int n = 0; n < 3; ++n) b[0][n] = ldstr2(lds + bo[0] + n * 56, 768);
#pragma unroll
  for (int i = 0; i < 4; ++i) a[0][i] = lds128(lds + ao[i]);
#pragma unroll
  for (int i = 1; i < 4; ++i)
#pragma unroll
    for (int n = 0; n < 3; ++n) b[i][n] = ldstr2(lds + bo[i] + n * 56, 768);
  SBAR();
#pragma unroll 1
  for (int st = 1; st < 7; st += 2) { stage(a[0], a[1], st); stage(a[1], a[0], st + 1); }
  STAMP(0, 7);
  dots(a[0]);
  float mu, rstd;
  lptr srow = lds + L_ROW + w * 128;
  u32x4 rf;
#pragma unroll
  for (int i = 0; i < 2; ++i)
#pragma unroll
    for (int n = 0; n < 3; ++n) {
      acc[n] = n < NQ ? MFMA(frag(b[i][n]), frag(a[0][i]), acc[n]) : MFMA(frag(a[0][i]), frag(b[i][n]), acc[n]);
      if (i == 0) {
        const float t = xhalf_sum(cs[n]);
        *(LAS float*)(lds + L_CS + (n * 32 + r) * 16 + w * 4) = t;
      } else if (n == 0) {
        mu = xhalf_sum(rs[0] + rs[1]) * (1.f / 448.f);
        PIN(mu);
      } else if (n == 1) {
        rstd = __builtin_amdgcn_rsqf(fmaxf(xhalf_sum(rq[0] + rq[1]) * (1.f / 448.f) - mu * mu, 0.f) + EPS);
        if (NQ < 3) *(LAS float*)(srow + r * 4) = rstd;
        PIN(rstd);
      } else {
        rf = rowfrag(-mu);
        PIN(rf);
      }
      SBAR();
    }
  BAR();
  f32x4 p4[3], r4[4];
  u32x4 cf[3];
#pragma unroll
  for (int n = 0; n < 3; ++n) p4[n] = ldsf4(lds + L_CS + (n * 32 + r) * 16);
  if (NQ < 3) {
#pragma unroll
    for (int g = 0; g < 4; ++g) r4[g] = ldsf4(srow + (8 * g + 4 * h) * 4);
  }
  SBAR();
#pragma unroll
  for (int i = 2; i < 4; ++i)
#pragma unroll
    for (int n = 0; n < 3; ++n) {
      acc[n] = n < NQ ? MFMA(frag(b[i][n]), frag(a[0][i]), acc[n]) : MFMA(frag(a[0][i]), frag(b[i][n]), acc[n]);
      if (i == 3) {
        float t0 = p4[n][0] + p4[n][1], t1 = p4[n][2] + p4[n][3];
        PIN(t0); PIN(t1);
        cf[n] = colfrag(t0 + t1, h);
        PIN(cf[n]);
      }
      SBAR();
    }
#pragma unroll
  for (int n = 0; n < 3; ++n) acc[n] = n < NQ ? MFMA(frag(cf[n]), frag(rf), acc[n]) : MFMA(frag(rf), frag(cf[n]), acc[n]);
  STAMP(0, 8);
  const int t = rb * 4 + w;
#pragma unroll
  for (int n = 0; n < 3; ++n) {
    const int u = cb * 3 + n;
    const float sc = u < 16 ? rstd * QSCALE : rstd;
#pragma unroll
    for (int s = 0; s < 2; ++s) {
      u32x4 o;
#pragma unroll
      for (int i = 0; i < 4; ++i) {
        const int e = 8 * s + 2 * i;
        o[i] = n < NQ ? pk2(acc[n][e] * sc, acc[n][e + 1] * sc) : pk2(acc[n][e] * r4[e >> 2][e & 3], acc[n][e + 1] * r4[e >> 2][(e & 3) + 1]);
      }
      if (n < NQ) {
        if (s == 1 && h) { o[2] = u < 16 ? 0u : ONES2; o[3] = u < 16 ? 0u : 0x00003F80u; }
      } else if (r >= 28) {
        o = (u32x4){ONES2, ONES2, ONES2, ONES2};
      }
      st16s(s, o, c.rws, lane * 16, (unsigned)(u * 64 + t) * 2048u);
    }
  }
  STAMP(0, 9);
  publish<4>(c, EV_PUB1, flag_off(0, cb * 16 + rb));
  STAMP(0, 10);
}

__device__ __forceinline__ void p1_load(const Ctx& c, const char* x, const char* wqkv, const char* wo, const char* w1, const char* w2, int rb, int cb) {
  const int lane = c.lane, w = c.w;
  const rsrc_t rx = mkrs(x, S_ * D_ * 2), rw = mkrs(wqkv, D_ * 3 * D_ * 2);
  unsigned vxa[2], vw[3];
#pragma unroll
  for (int p = 0; p < 2; ++p) vxa[p] = (unsigned)(lane >> 3) * 896u + (unsigned)((lane & 7) ^ ((4 * p + (lane >> 4)) & 7)) * 16u;
#pragma unroll
  for (int q = 0; q < 3; ++q) { const int i = q * 64 + lane; vw[q] = (unsigned)(i / 12) * 2688u + (unsigned)(i % 12) * 16u; }
  const unsigned sx0 = (unsigned)(rb * 128 + w * 32) * 896u, sw0 = (((unsigned)cb * 168u) & ~15u) + (unsigned)w * (16u * 2688u);
  int ks = (cb + 2 * rb) % 7;
#pragma unroll 1
  for (int st = 0; st < 7; ++st) {
    if (st >= 3) WAITV_BAR(14);
    const unsigned lb = c.lbase + (st >= 5 ? st - 5 : st) * P1_SLOT;
#pragma unroll
    for (int p = 0; p < 4; ++p) dma16(rx, vxa[p & 1], sx0 + ks * 128u + p * 7168u, lb + (w * 4 + p) * 1024);
#pragma unroll
    for (int q = 0; q < 3; ++q) dma16(rw, vw[q], sw0 + ks * (64u * 2688u), lb + 16384 + (w * 3 + q) * 1024);
    ks = ks == 6 ? 0 : ks + 1;
  }
  const rsrc_t rt = mkrs(w == 0 ? wo : (w == 1 ? w1 : w2), w == 0 ? D_ * D_ * 2 : (w < 3 ? D_ * DFF * 2 : 0));
  touch_lds(c, rt, lane * 128, blockIdx.x * (w == 0 ? 8192u : 6272u));
  WAITV_BAR(15);
  WAITV_BAR(8);
  WAITV_BAR(1);
  BAR();
}

#define ATT_UNIT(SC, SN, KN0, KN1, QN, OPREV, VPREV1, OCUR, VCUR0, HOOK_B, HOOK_C, HOOK_D)               \
  do {                                                                                                     \
    SN = MFMA(frag(KN0), frag(QN[0]), zero); SBAR();                                                       \
    p1[2] = pk2(e[4], e[5]); p1[3] = pk2(e[6], e[7]); PIN(p1); SBAR();                                     \
    e[0] = EX(SC[0]); e[1] = EX(SC[1]); e[2] = EX(SC[2]); e[3] = EX(SC[3]);                                \
    PIN(e); SBAR();                                                                                        \
    OPREV = MFMA(frag(VPREV1), frag(p1), OPREV); SBAR();                                                   \
    p0[0] = pk2(e[0], e[1]); p0[1] = pk2(e[2], e[3]); PIN(p0); SBAR();                                     \
    e[4] = EX(SC[4]); e[5] = EX(SC[5]); e[6] = EX(SC[6]); e[7] = EX(SC[7]);                                \
    PIN(e); SBAR(); HOOK_B;                                                                                \
    SN = MFMA(frag(KN1), frag(QN[1]), SN); SBAR();                                                         \
    p0[2] = pk2(e[4], e[5]); p0[3] = pk2(e[6], e[7]); PIN(p0); SBAR();                                     \
    e[0] = EX(SC[8]); e[1] = EX(SC[9]); e[2] = EX(SC[10]); e[3] = EX(SC[11]);                              \
    PIN(e); SBAR(); HOOK_C;                                                                                \
    OCUR = MFMA(frag(VCUR0), frag(p0), OCUR); SBAR();                                                      \
    p1[0] = pk2(e[0], e[1]); p1[1] = pk2(e[2], e[3]); PIN(p1); SBAR();                                     \
    e[4] = EX(SC[12]); e[5] = EX(SC[13]); e[6] = EX(SC[14]); e[7] = EX(SC[15]);                            \
    PIN(e); SBAR(); HOOK_D;                                                                                \
  } while (0)

__device__ __forceinline__ int p4_rot(int cb4) { return cb4 >= 7 ? cb4 - 7 : cb4; }
__device__ __forceinline__ unsigned w1_piece(int p, int cb4) {
  const int q = p + p4_rot(cb4) * 16;
  return (unsigned)(q >= 112 ? q - 112 : q) * 14336u + cb4 * 256u;
}
struct KvRegs { u32x4 ka0, ka1, va0, va1, kb0, kb1, vb0; };

template <bool EXACT>
__device__ __forceinline__ void attn_pass(const Ctx& c, rsrc_t rt, KvRegs& kv) {
  const int lane = c.lane, w = c.w8, r = c.r, h = c.h;
  lptr lds = c.lds;
  const int hd = (blockIdx.x & 7) * 2 + ((blockIdx.x >> 3) & 1), rb = blockIdx.x >> 4, j = blockIdx.x >> 3;
  const int qp = w & 1, kq = w >> 1;
  const unsigned vo = lane * 16, vt = lane * 64;
  const unsigned sq = OFF_Q + (unsigned)(hd * 64 + rb * 4 + qp * 2) * 2048u;
  const unsigned sk = OFF_K + (unsigned)(hd * 64 + kq * 16) * 2048u, sv = sk + QKV_BYTES;
  const unsigned rot = (unsigned)(rb & 7) * 4096u;
  const rsrc_t rs = c.rws;
  const f32x16 zero = {};
  u32x4 &ka0 = kv.ka0, &ka1 = kv.ka1, &va0 = kv.va0, &va1 = kv.va1, &kb0 = kv.kb0, &kb1 = kv.kb1, &vb0 = kv.vb0;
  u32x4 qa[2], qb[2], vb1;
  float m[2] = {-INFINITY, -INFINITY};
#pragma unroll 1
  for (int i = EXACT ? 15 : 0; i >= 0; --i) {
    const unsigned ko = sk + ((rot + i * 2048u) & 32767u);
    ld16<0>(qa[0], rs, vo, sq); ld16<0>(ka0, rs, vo, ko); ld16<2048>(qb[0], rs, vo, sq);
    ld16<1024>(qa[1], rs, vo, sq); ld16<1024>(ka1, rs, vo, ko); ld16<3072>(qb[1], rs, vo, sq);
    ld16<0>(va0, rs, vo, sv + rot); ld16<1024>(va1, rs, vo, sv + rot);
    ld16<2048>(kb0, rs, vo, sk + rot); ld16<3072>(kb1, rs, vo, sk + rot);
    ld16<2048>(vb0, rs, vo, sv + rot);
    asm volatile("s_waitcnt vmcnt(5)" : "+v"(qa[0]), "+v"(qa[1]), "+v"(qb[0]), "+v"(qb[1]), "+v"(ka0), "+v"(ka1) :: "memory");
    f32x16 s0 = MFMA(frag(ka0), frag(qa[0]), zero), s1 = MFMA(frag(ka0), frag(qb[0]), zero);
    s0 = MFMA(frag(ka1), frag(qa[1]), s0); s1 = MFMA(frag(ka1), frag(qb[1]), s1);
#pragma unroll
    for (int e = 0; e < 16; ++e) { m[0] = fmaxf(m[0], s0[e]); m[1] = fmaxf(m[1], s1[e]); }
  }
#pragma unroll
  for (int b = 0; b < 2; ++b) {
    m[b] = xhalf_max(m[b]);
    const float nm = -m[b];
    const unsigned u0 = __float_as_uint(nm) & 0xffff0000u;
    const float r1 = nm - __uint_as_float(u0);
    const unsigned u1 = __float_as_uint(r1) & 0xffff0000u;
    const float r2 = r1 - __uint_as_float(u1);
    u32x4& qq = b ? qb[1] : qa[1];
    qq[2] = h ? ((u0 >> 16) | u1) : qq[2];
    qq[3] = h ? (__float_as_uint(r2) >> 16) : qq[3];
  }
  STAMP(1, w ? 17 : 1);
  f32x16 o0 = zero, o1 = zero;
  f32x16 sa = MFMA(frag(ka0), frag(qa[0]), zero), sb;
  sa = MFMA(frag(ka1), frag(qa[1]), sa);
  u32x4 p0 = {}, p1 = {};
  f32x8 e = {};
  vb1 = (u32x4){};
  touch_lds(c, rt, vt, OOB);
  unsigned oc = rot, to = w < 4 ? (unsigned)(j * 2 + (w & 1)) * 25088u : (unsigned)j * 12544u;
#pragma unroll 1
  for (int i = 0; i < 8; ++i) {
    const unsigned on = (oc + 4096u) & 32767u;
    const unsigned kn = i < 7 ? sk + on : OOB, vn = i < 7 ? sv + on : OOB;
    ATT_UNIT(sa, sb, ka0, ka1, qb, o1, vb1, o0, va0,
             ld16<3072>(vb1, rs, vo, sv + oc),
             { ld16<0>(ka0, rs, vo, kn); ld16<1024>(ka1, rs, vo, kn);
               asm volatile("s_waitcnt vmcnt(7)" : "+v"(va0), "+v"(va1) :: "memory"); },
             asm volatile("s_waitcnt vmcnt(5)" : "+v"(kb0), "+v"(kb1) :: "memory"));
    ATT_UNIT(sb, sa, kb0, kb1, qa, o0, va1, o1, va0,
             (void)0, (void)0,
             ld16<0>(va0, rs, vo, vn));
    ATT_UNIT(sa, sb, kb0, kb1, qb, o1, va1, o0, vb0,
             ld16<1024>(va1, rs, vo, vn),
             { ld16<2048>(kb0, rs, vo, kn); ld16<3072>(kb1, rs, vo, kn);
               asm volatile("s_waitcnt vmcnt(6)" : "+v"(vb0), "+v"(vb1) :: "memory"); },
             asm volatile("s_waitcnt vmcnt(4)" : "+v"(ka0), "+v"(ka1) :: "memory"));
    ATT_UNIT(sb, sa, ka0, ka1, qa, o0, vb1, o1, vb0,
             (void)0, (void)0,
             { ld16<2048>(vb0, rs, vo, vn); touch_lds(c, rt, vt, to); });
    oc = on; to += 4096u;
  }
  p1[2] = pk2(e[4], e[5]); p1[3] = pk2(e[6], e[7]);
  o1 = MFMA(frag(vb1), frag(p1), o1);
  STAMP(1, w ? 18 : 2);
#pragma unroll
  for (int f = 0; f < 4; ++f) {
    const f32x16& o = (f >> 1) ? o1 : o0;
    const int s = f & 1;
    lptr dst = lds + ((qp * 4 + (f >> 1) + 2 * s) * 4 + kq) * 2048 + lane * 16;
    *(LAS f32x4*)dst = (f32x4){o[8 * s], o[8 * s + 1], o[8 * s + 2], o[8 * s + 3]};
    *(LAS f32x4*)(dst + 1024) = (f32x4){o[8 * s + 4], o[8 * s + 5], o[8 * s + 6], o[8 * s + 7]};
  }
  bool bad = false;
#pragma unroll
  for (int b = 0; b < 2; ++b) {
    const float l = b ? o1[12] : o0[12];
    if (h) {
      *(LAS f32x2*)(lds + L_ML + ((qp * 2 + b) * 32 + r) * 32 + kq * 8) = (f32x2){m[b], l};
      bad |= !(l >= 0x1p-30f && l <= 0x1p30f);
    }
  }
  if (!EXACT && bad) *(LAS unsigned*)(lds + L_CNT + EV_REDO * 16) = 1;
}
#define KV_DRAIN(kv) asm volatile("s_waitcnt vmcnt(0)" : "+v"(kv.ka0), "+v"(kv.ka1), "+v"(kv.va0), "+v"(kv.va1), "+v"(kv.kb0), "+v"(kv.kb1), "+v"(kv.vb0) :: "memory")

__device__ __forceinline__ void p2_attn(const Ctx& c, const char* wo, const char* w1, const char* w2, int cb3, int cb4, u32x4 (&bw)[16][2]) {
  const int lane = c.lane, w = c.w8, r = c.r, h = c.h;
  lptr lds = c.lds;
  const int hd = (blockIdx.x & 7) * 2 + ((blockIdx.x >> 3) & 1), rb = blockIdx.x >> 4, j = blockIdx.x >> 3;
  const int qp = w & 1, kq = w >> 1;
  const rsrc_t rt = mkrs(w < 2 ? w1 : (w < 4 ? w2 : wo), w < 4 ? D_ * DFF * 2 : (w == 4 ? (j + 1) * 12544 : 0));
  if (w == 0) {
    const int u = lane < 16 ? 16 + hd : (lane < 32 ? 32 + hd : hd);
    poll(c, flag_off(0, (u / 3) * 16 + (lane < 32 ? (lane & 15) : rb)));
  }
  BAR();
  STAMP(1, w ? 16 : 0);
  KvRegs kv;
  if (w < 4) __builtin_amdgcn_s_setprio(1);
  attn_pass<false>(c, rt, kv);
  __builtin_amdgcn_s_setprio(0);
  KV_DRAIN(kv);
  const bool pre = w < 4 && j < 28;
  const rsrc_t rwo = mkrs(wo, D_ * D_ * 2), rw1 = mkrs(w1, D_ * DFF * 2);
  const unsigned vwo = (unsigned)(lane >> 3) * 896u + (unsigned)((lane & 7) ^ (((lane >> 4) & 1) << 2)) * 16u;
  const unsigned vw1 = (unsigned)(lane >> 4) * 3584u + (unsigned)((lane & 15) ^ ((lane >> 4) << 2)) * 16u;
  auto dma_wo = [&]() {
#pragma unroll 1
    for (int p = w; p < 57; p += 4) dma16(rwo, vwo, p * 7168u + cb3 * 128u, c.lbase + L_WO + p * 1024);
  };
  auto dma_w1 = [&](int p0, int p1) {
#pragma unroll 1
    for (int p = p0 + w; p < p1; p += 4) dma16(rw1, vw1, w1_piece(p, cb4), c.lbase + 4 * SLOT + p * 1024);
  };
  auto read_wo = [&]() {
    const int q = (lane & 15) >> 2, kh = (w >> 1) & 1;
    const unsigned b0 = L_WO + (kh * 224 + 4 * h + q) * 128 + ((lane >> 4) & 1) * 32 + (lane & 3) * 8;
    unsigned bb[2] = {b0 + ((kh ^ (q >> 1)) * 64), b0 + ((kh ^ 1 ^ (q >> 1)) * 64)};
    PIN(bb[0]); PIN(bb[1]);
#pragma unroll
    for (int ks = 0; ks < 16; ++ks)
#pragma unroll
      for (int i = 0; i < 2; ++i) bw[ks][i] = ldstr2(lds + bb[i] + ((ks >> 1) * 28 + (ks & 1) * 16) * 128, 1024);
  };
  if (pre) {
    dma_wo();
    dma_w1(0, 32);
    asm volatile("s_waitcnt vmcnt(8)" ::: "memory");
    ev_set(c, EV_WO, w);
    ev_wait<4>(c, EV_WO);
    read_wo();
    ev_set(c, EV_BPRE, w);
    ev_wait<4>(c, EV_BPRE);
    dma_w1(32, 80);
    STAMP(1, 3);
  }
  BAR();
  if (__builtin_amdgcn_readfirstlane(*(LAS unsigned*)(lds + L_CNT + EV_REDO * 16))) {
    WAITV_BAR(0);
    attn_pass<true>(c, rt, kv);
    KV_DRAIN(kv);
    if (pre) dma_wo();
    WAITV_BAR(0);
    read_wo();
    BAR();
    if (pre) dma_w1(32, 80);
    WAITV_BAR(0);
  }
  STAMP(1, w ? 19 : 4);
  lptr mlp = lds + L_ML + ((qp * 2 + (kq & 1)) * 32 + r) * 32;
  const f32x4 a0 = ldsf4(mlp), a1 = ldsf4(mlp + 16);
  f32x4 lo[4], hi[4];
#pragma unroll
  for (int k = 0; k < 4; ++k) {
    lptr src = lds + ((qp * 4 + kq) * 4 + k) * 2048 + lane * 16;
    lo[k] = ldsf4(src); hi[k] = ldsf4(src + 1024);
  }
  const float M = fmaxf(fmaxf(a0[0], a0[2]), fmaxf(a1[0], a1[2]));
  float wk[4] = {EX(a0[0] - M), EX(a0[2] - M), EX(a1[0] - M), EX(a1[2] - M)};
  const float inv = __builtin_amdgcn_rcpf((a0[1] * wk[0] + a0[3] * wk[1]) + (a1[1] * wk[2] + a1[3] * wk[3]));
#pragma unroll
  for (int k = 0; k < 4; ++k) wk[k] *= inv;
  const f32x4 fl = (lo[0] * wk[0] + lo[1] * wk[1]) + (lo[2] * wk[2] + lo[3] * wk[3]);
  const f32x4 fh = (hi[0] * wk[0] + hi[1] * wk[1]) + (hi[2] * wk[2] + hi[3] * wk[3]);
  u32x4 res = {pk2(fl[0], fl[1]), pk2(fl[2], fl[3]), pk2(fh[0], fh[1]), pk2(fh[2], fh[3])};
  if (h && (kq >> 1)) { res[2] = 0; res[3] = 0; }
  const int t = rb * 4 + qp * 2 + (kq & 1);
  st16<0>(res, c.rws, lane * 16, OFF_O + (unsigned)((t * 16 + hd) * 2 + (kq >> 1)) * 1024u);
  ev_set(c, EV_CMB0, w);
  STAMP(1, w ? 20 : 5);
}
__device__ __forceinline__ void p2_publish(const Ctx& c) {
  publish<8>(c, EV_PUB2, flag_off(1, (blockIdx.x >> 4) * 16 + (blockIdx.x & 7) * 2 + ((blockIdx.x >> 3) & 1)));
}

__device__ __forceinline__ void stats_issue(const Ctx& c, int t, f32x2 (&v)[7]) {
  const unsigned vo = (c.h * 7 * 2048 + c.r) * 8;
#pragma unroll
  for (int i = 0; i < 7; ++i)
    asm volatile("buffer_load_dwordx2 %0, %1, %2, %3 offen sc1" : "=v"(v[i]) : "v"(vo), "s"(c.rws), "s"(OFF_ST + (unsigned)(i * 2048 + t * 32) * 8u) : "memory");
}
#define STATS_WAIT(N, v) asm volatile("s_waitcnt vmcnt(" #N ")" : "+v"(v[0]), "+v"(v[1]), "+v"(v[2]), "+v"(v[3]), "+v"(v[4]), "+v"(v[5]), "+v"(v[6]) :: "memory")
__device__ __forceinline__ void stats_pin(f32x2 (&v)[7]) { asm volatile("" : "+v"(v[0]), "+v"(v[1]), "+v"(v[2]), "+v"(v[3]), "+v"(v[4]), "+v"(v[5]), "+v"(v[6])); }
__device__ __forceinline__ void stats_finish(const f32x2 (&v)[7], float& mu, float& rstd) {
  const f32x2 t = ((v[0] + v[1]) + (v[2] + v[3])) + ((v[4] + v[5]) + v[6]);
  mu = xhalf_sum(t[0]) * (1.f / 448.f);
  rstd = __builtin_amdgcn_rsqf(fmaxf(xhalf_sum(t[1]) * (1.f / 448.f) - mu * mu, 0.f) + EPS);
}
__device__ __forceinline__ unsigned relu2(float a, float b) {
  return __builtin_bit_cast(unsigned, __builtin_elementwise_max(__builtin_bit_cast(s16x2, pk2(a, b)), (s16x2){0, 0}));
}
constexpr int w1_slot(int st) { return st < 5 ? 4 + st : st - 5; }
constexpr int w2_pos(int k) { return k < 35 ? k : k - 35; }

__device__ __forceinline__ void pair_reduce(const Ctx& c, f32x16 (&acc)[2], int lbuf, int ev) {
  lptr dst = c.lds + lbuf + c.w * 4096 + c.lane * 16;
#pragma unroll
  for (int g = 0; g < 4; ++g) *(LAS f32x4*)(dst + g * 1024) = (f32x4){acc[1][4 * g], acc[1][4 * g + 1], acc[1][4 * g + 2], acc[1][4 * g + 3]};
  ev_set(c, ev, c.w);
  PIN(acc[0]);
  const unsigned fa = c.lbase + L_CNT + ev * 16 + (c.w ^ 2) * 4, sa = c.lbase + lbuf + (c.w ^ 2) * 4096 + c.lane * 16;
  f32x4 v0, v1, v2, v3;
#pragma unroll 1
  for (int spins = 0; spins < (1 << 24); ++spins) {
    unsigned f;
    asm volatile("ds_read_b32 %0, %5\n\tds_read_b128 %1, %6\n\tds_read_b128 %2, %6 offset:1024\n\tds_read_b128 %3, %6 offset:2048\n\t"
                 "ds_read_b128 %4, %6 offset:3072\n\ts_waitcnt lgkmcnt(0)"
                 : "=&v"(f), "=&v"(v0), "=&v"(v1), "=&v"(v2), "=&v"(v3) : "v"(fa), "v"(sa) : "memory");
    if (__builtin_amdgcn_readfirstlane(f)) break;
  }
#pragma unroll
  for (int e = 0; e < 4; ++e) { acc[0][e] += v0[e]; acc[0][4 + e] += v1[e]; acc[0][8 + e] += v2[e]; acc[0][12 + e] += v3[e]; }
}

__device__ __forceinline__ void p345_compute(const Ctx& c, const u32x2 (&xr)[4], u32x4 (&bw)[16][2], char* out, int rb3, int cb3, int rb4, int cb4) {
  const int lane = c.lane, w = c.w, r = c.r, h = c.h;
  lptr lds = c.lds;
  const rsrc_t rs = c.rws;
  const int wr = w & 1, kh = w >> 1, q = (lane & 15) >> 2;
  const int t3 = rb3 * 2 + wr, sub = cb3 * 2 + kh;
  const unsigned trl = ((lane >> 4) & 1) * 32 + (lane & 3) * 8, va = lane * 16;
  u32x4 hres[2];
  {
    f32x16 acc[2];
#pragma unroll
    for (int g = 0; g < 4; ++g)
#pragma unroll
      for (int i = 0; i < 2; ++i) { acc[0][4 * g + 2 * i] = bflo(xr[g][i]); acc[0][4 * g + 2 * i + 1] = bfhi(xr[g][i]); }
    acc[1] = f32x16{};
    pin16(acc[0]); pin16(acc[1]);
    p2_publish(c);
    ev_set(c, EV_W1A, w);
    STAMP(1, 6);
    ev_wait<4>(c, EV_CMB0); ev_wait<4>(c, EV_CMB1);
    acquire(c, flag_off(1, (rb3 >> 1) * 16 + (lane & 15)), EV_GO3);
    STAMP(2, 0);
    const unsigned sa = OFF_O + (unsigned)(t3 * 32 + kh * 16) * 1024u;
    u32x4 a[16];
    static_for<0, 10>([&](auto kc) { constexpr int k = decltype(kc)::value; ld16<(k & 3) * 1024>(a[k], rs, va, sa + (k >> 2) * 4096u); });
    static_for<0, 16>([&](auto kc) {
      constexpr int k = decltype(kc)::value;
      waitv<(k <= 6 ? 9 : 15 - k)>(a[k]);
      acc[1] = MFMA(frag(bw[k][1]), frag(a[k]), acc[1]);
      SBAR();
      if constexpr (k + 10 < 16) ld16<((k + 10) & 3) * 1024>(a[k + 10], rs, va, sa + ((k + 10) >> 2) * 4096u);
      SBAR();
      acc[0] = MFMA(frag(bw[k][0]), frag(a[k]), acc[0]);
      SBAR();
    });
    STAMP(2, 7);
    pair_reduce(c, acc, L_RED3, EV_RED3);
    STAMP(2, 8);
#pragma unroll
    for (int i = 0; i < 8; ++i) hres[i >> 2][i & 3] = pk2(acc[0][2 * i], acc[0][2 * i + 1]);
#pragma unroll
    for (int sx = 0; sx < 2; ++sx) st16s(sx, hres[sx], rs, va, OFF_H + (unsigned)(t3 * 28 + sub * 2) * 1024u);
    f32x4 s4 = {}, q4 = {};
#pragma unroll
    for (int g = 0; g < 4; ++g) {
      const f32x4 v = {acc[0][4 * g], acc[0][4 * g + 1], acc[0][4 * g + 2], acc[0][4 * g + 3]};
      s4 += v; q4 += v * v;
    }
    const float s = xhalf_sum((s4[0] + s4[1]) + (s4[2] + s4[3])), sq = xhalf_sum((q4[0] + q4[1]) + (q4[2] + q4[3]));
    if (h == 0) __builtin_amdgcn_raw_buffer_store_b64((u32x2){__float_as_uint(s), __float_as_uint(sq)}, rs, r * 8, OFF_ST + (unsigned)(sub * 2048 + t3 * 32) * 8u, 16);
    ev_set(c, EV_R3DONE, w);
    STAMP(2, 9);
  }
  {
    const int t4 = rb4 * 4 + w;
    unsigned bo[4][3];
#pragma unroll
    for (int i = 0; i < 4; ++i) {
      bo[i][0] = (4 * h + q) * 256 + ((i ^ q) * 64) + trl; bo[i][1] = bo[i][0] + 65536; bo[i][2] = bo[i][0] + 131072;
      PIN(bo[i][0]); PIN(bo[i][1]); PIN(bo[i][2]);
    }
    ev_wait<4>(c, EV_CS);
    u32x2 cf[4];
#pragma unroll
    for (int i = 0; i < 4; ++i) { const u32x4 t = colfrag(*(LAS float*)(lds + L_CS + i * 128 + r * 4), h); cf[i] = (u32x2){t[0], t[1]}; PIN(cf[i]); }
    u32x4 a[28], bq[4][2];
    auto bread = [&](auto kc) {
      constexpr int k = decltype(kc)::value, pass = k / 28, ks = k % 28, off = w1_slot(ks >> 2) * SLOT + (ks & 3) * 4096;
      bq[k & 3][0] = ldstr2(lds + bo[2 * pass][off >> 16] + (off & 65535), 2048);
      bq[k & 3][1] = ldstr2(lds + bo[2 * pass + 1][off >> 16] + (off & 65535), 2048);
    };
    bread(IC<0>{}); bread(IC<1>{}); bread(IC<2>{});
    SBAR();
    publish<4>(c, EV_PUB3, flag_off(2, rb3 * 8 + cb3));
    STAMP(2, 10);
    { const int l = lane < 14 ? lane : 0; acquire(c, flag_off(2, (rb4 * 2 + (l >= 7)) * 8 + (l >= 7 ? l - 7 : l)), EV_GO4); }
    STAMP(3, 0);
    const int rot = p4_rot(cb4);
    const unsigned sa0 = OFF_H + (unsigned)(t4 * 28 + rot * 4) * 1024u;
    auto sa = [&](int g) { return sa0 + g * 4096u - (g + rot >= 7 ? 28672u : 0u); };
    const unsigned sg = OFF_G + (unsigned)(t4 * 112 + cb4 * 8) * 1024u;
    f32x2 sv[7];
    static_for<0, 12>([&](auto kc) { constexpr int k = decltype(kc)::value; ld16<(k & 3) * 1024>(a[k], rs, va, sa(k >> 2)); });
    f32x16 acc[4] = {};
    u32x4 og, rf;
    static_for<0, 56>([&](auto kc) {
      constexpr int k = decltype(kc)::value, pass = k / 28, ks = k % 28;
      if constexpr (k < 28) waitv<((k + 11 < 27 ? k + 11 : 27) - k + (k >= 9 && k <= 20 ? 7 : 0))>(a[k]);
      if constexpr (k == 28) {
#pragma unroll
        for (int i = 0; i < 4; ++i) acc[i] = MFMA(frag((u32x4){cf[i][0], cf[i][1], 0u, 0u}), frag(rf), acc[i]);
        SBAR();
      }
      acc[2 * pass] = MFMA(frag(bq[k & 3][0]), frag(a[ks]), acc[2 * pass]);
      SBAR();
      if constexpr (k + 12 < 28) ld16<((k + 12) & 3) * 1024>(a[k + 12], rs, va, sa((k + 12) >> 2));
      if constexpr (k == 8) stats_issue(c, t4, sv);
      if constexpr (k + 3 < 56) bread(IC<k + 3>{});
      SBAR();
      acc[2 * pass + 1] = MFMA(frag(bq[k & 3][1]), frag(a[ks]), acc[2 * pass + 1]);
      SBAR();
      if constexpr (k == 21) {
        stats_pin(sv);
        float t = sv[0][0];
#pragma unroll
        for (int i = 1; i < 7; ++i) { t += sv[i][0]; pinf(t); }
        rf = rowfrag(xhalf_sum(t) * (-1.f / 448.f));
        pin4(rf);
        SBAR();
      }
      if constexpr (k == 24) { ev_set(c, EV_A4, w); SBAR(); }
      if constexpr (k >= 30 && k < 46) {
        constexpr int e = k - 30;
        og[e & 3] = relu2(acc[e >> 3][(e & 7) * 2], acc[e >> 3][(e & 7) * 2 + 1]);
        PIN(og);
        if constexpr ((e & 3) == 3) st16<(e >> 2) * 1024>(og, rs, va, sg);
        SBAR();
      }
    });
    ev_set(c, EV_X4, w);
    STAMP(3, 8);
    static_for<0, 4>([&](auto fc) {
      constexpr int f = decltype(fc)::value;
      u32x4 o;
#pragma unroll
      for (int e = 0; e < 4; ++e) o[e] = relu2(acc[2 + (f >> 1)][8 * (f & 1) + 2 * e], acc[2 + (f >> 1)][8 * (f & 1) + 2 * e + 1]);
      st16<f * 1024>(o, rs, va, sg + 4096u);
    });
    STAMP(3, 9);
    publish<4>(c, EV_PUB4, flag_off(3, rb4 * 16 + cb4));
    STAMP(3, 10);
  }
  {
    unsigned bo[2][3];
#pragma unroll
    for (int i = 0; i < 2; ++i) {
      bo[i][0] = kh * 2048 + (4 * h + q) * 128 + ((kh ^ i ^ (q >> 1)) * 64) + trl; bo[i][1] = bo[i][0] + 65536; bo[i][2] = bo[i][0] + 131072;
      PIN(bo[i][0]); PIN(bo[i][1]); PIN(bo[i][2]);
    }
    acquire(c, flag_off(3, (rb3 >> 1) * 16 + (lane < 14 ? lane : 0)), EV_GO5);
    STAMP(4, 0);
    const unsigned sa0 = OFF_G + (unsigned)(t3 * 112 + kh * 56 + cb3 * 8) * 1024u;
    auto sa = [&](int g) { return sa0 + g * 4096u - (g + cb3 * 2 >= 14 ? 57344u : 0u); };
    u32x4 a[12], bq[4][2];
    f32x2 sv[7];
    static_for<0, 12>([&](auto kc) { constexpr int k = decltype(kc)::value; ld16<(k & 3) * 1024>(a[k], rs, va, sa(k >> 2)); });
    auto bread = [&](auto kc) {
      constexpr int k = decltype(kc)::value, off = w2_pos(k) * 4096;
      bq[k & 3][0] = ldstr2(lds + bo[0][off >> 16] + (off & 65535), 1024);
      bq[k & 3][1] = ldstr2(lds + bo[1][off >> 16] + (off & 65535), 1024);
    };
    ev_wait_half(c, EV_W2A);
    bread(IC<0>{}); bread(IC<1>{}); bread(IC<2>{});
    f32x16 acc[2] = {};
    static_for<0, 56>([&](auto kc) {
      constexpr int k = decltype(kc)::value;
      waitv<(k <= 44 ? 11 : 55 - k + 7)>(a[k % 12]);
      acc[1] = MFMA(frag(bq[k & 3][1]), frag(a[k % 12]), acc[1]);
      SBAR();
      if constexpr (k == 4 || k == 11 || k == 18) ev_set(c, EV_SEG0 + (k - 4) / 7, w);
      if constexpr (k == 32) { pin16(acc[0]); pin16(acc[1]); ev_wait_half(c, EV_W2B); }
      if constexpr (k + 3 < 56) bread(IC<k + 3>{});
      SBAR();
      acc[0] = MFMA(frag(bq[k & 3][0]), frag(a[k % 12]), acc[0]);
      SBAR();
      if constexpr (k + 12 < 56) ld16<((k + 12) & 3) * 1024>(a[k % 12], rs, va, sa((k + 12) >> 2));
      if constexpr (k == 44) stats_issue(c, t3, sv);
      SBAR();
    });
    STAMP(4, 15);
    float mu, rstd;
    STATS_WAIT(0, sv);
    stats_finish(sv, mu, rstd);
    pair_reduce(c, acc, L_RED5, EV_RED5);
    STAMP(4, 16);
#pragma unroll
    for (int sx = 0; sx < 2; ++sx) {
      u32x4 o;
#pragma unroll
      for (int i = 0; i < 4; ++i) o[i] = pk2(acc[0][8 * sx + 2 * i] * rstd + bflo(hres[sx][i]), acc[0][8 * sx + 2 * i + 1] * rstd + bfhi(hres[sx][i]));
      auto x0 = __builtin_amdgcn_permlane32_swap(o[0], o[2], false, false);
      auto x1 = __builtin_amdgcn_permlane32_swap(o[1], o[3], false, false);
      *(u32x4*)(out + (unsigned)(t3 * 32 + r) * 896u + (sub * 32 + 16 * sx + 8 * h) * 2) = (u32x4){x0[0], x1[0], x0[1], x1[1]};
    }
    STAMP(4, 18);
  }
}

__device__ __forceinline__ void p345_load(const Ctx& c, const char* w1, const char* w2, int cb3, int cb4) {
  const int lane = c.lane, w = c.w;
  const rsrc_t rw1 = mkrs(w1, D_ * DFF * 2), rw2 = mkrs(w2, D_ * DFF * 2);
  const unsigned vw1 = (unsigned)(lane >> 4) * 3584u + (unsigned)((lane & 15) ^ ((lane >> 4) << 2)) * 16u;
  const unsigned vw2 = (unsigned)(lane >> 3) * 896u + (unsigned)((lane & 7) ^ (((lane >> 4) & 1) << 2)) * 16u;
  const unsigned sw0 = (unsigned)((w >> 1) * 896 + (w & 1) * 8 + cb3 * 128) * 896u + cb3 * 128u;
  auto sw = [&](int k) { return sw0 + k * 14336u - (k + cb3 * 8 >= 56 ? 56u * 14336u : 0u); };
  u32x4 st[48];
  auto ldw = [&](auto ic) { constexpr int i = decltype(ic)::value, k = i < 8 ? i : i + 8; ld16p(st[i], rw2, vw2, sw(k)); };
  p2_publish(c);
  ev_wait<4>(c, EV_CMB0); ev_wait<4>(c, EV_CMB1);
#pragma unroll 1
  for (int p = w; p < 32; p += 4) dma16(rw1, vw1, w1_piece(80 + p, cb4), c.lbase + p * 1024);
  static_for<0, 8>(ldw);
  asm volatile("s_waitcnt vmcnt(8)" ::: "memory");
  ev_set(c, EV_W1, w);
  ev_wait<4>(c, EV_W1); ev_wait<4>(c, EV_W1A);
  {
    const int q = (lane & 15) >> 2;
    lptr bp = c.lds + (4 * c.h + q) * 256 + ((w ^ q) * 64) + ((lane >> 4) & 1) * 32 + (lane & 3) * 8;
    float cs[4] = {0.f, 0.f, 0.f, 0.f};
#pragma unroll 1
    for (int sl = 0; sl < 7; ++sl) {
      u32x2 b[8];
#pragma unroll
      for (int k = 0; k < 8; ++k) b[k] = ldstr(bp + (sl < 2 ? sl : sl + 2) * SLOT + k * 2048);
      SBAR();
#pragma unroll
      for (int k = 0; k < 8; ++k) { cs[(k & 1) * 2] = dot2(b[k][0], ONES2, cs[(k & 1) * 2]); cs[(k & 1) * 2 + 1] = dot2(b[k][1], ONES2, cs[(k & 1) * 2 + 1]); }
    }
    *(LAS float*)(c.lds + L_CS + w * 128 + c.r * 4) = xhalf_sum((cs[0] + cs[1]) + (cs[2] + cs[3]));
    ev_set(c, EV_CS, w);
  }
  ev_wait<4, 1>(c, EV_R3DONE);
  static_for<8, 30>(ldw);
  ev_wait<4, 2>(c, EV_A4);
#pragma unroll 1
  for (int k = 8; k < 16; ++k) dma16(rw2, vw2, sw(k), c.lbase + k * 4096 + w * 1024);
  static_for<30, 48>(ldw);
  unsigned wb[3] = {(unsigned)(w * 1024 + lane * 16), 0u, 0u};
  wb[1] = wb[0] + 65536; wb[2] = wb[0] + 131072;
  PIN(wb[0]); PIN(wb[1]); PIN(wb[2]);
  auto stw = [&](auto ic) {
    constexpr int i = decltype(ic)::value, k = i < 8 ? i : i + 8, off = w2_pos(k) * 4096;
    waitv<(i < 30 ? 18 : 47 - i)>(st[i]);
    *(LAS u32x4*)(c.lds + wb[off >> 16] + (off & 65535)) = st[i];
  };
  ev_wait<4, 2>(c, EV_X4);
  static_for<0, 27>(stw);
  ev_set(c, EV_W2A, w);
  ev_wait_half<2>(c, EV_SEG0);
  static_for<27, 34>(stw);
  ev_wait_half<2>(c, EV_SEG1);
  static_for<34, 41>(stw);
  ev_wait_half<2>(c, EV_SEG2);
  static_for<41, 48>(stw);
  ev_set(c, EV_W2B, w);
}

__global__ __launch_bounds__(512, 2) void block_fwd(const char* __restrict__ x, const char* __restrict__ wqkv, const char* __restrict__ wo,
                                                    const char* __restrict__ w1, const char* __restrict__ w2,
                                                    char* __restrict__ ws, char* __restrict__ out) {
  extern __shared__ __attribute__((aligned(16))) char smem[];
  unsigned long long pc;
  asm volatile("s_getpc_b64 %0" : "=s"(pc));
  asm volatile("" :: "s"(x), "s"(wqkv), "s"(wo), "s"(w1), "s"(w2), "s"(ws), "s"(out));
  Ctx c;
  c.lds = (lptr)smem; c.lbase = (unsigned)(uintptr_t)smem; c.ws = ws;
  c.rws = mkrs(ws, WS_USED);
  c.lane = threadIdx.x & 63; c.w8 = __builtin_amdgcn_readfirstlane(threadIdx.x >> 6); c.w = c.w8 & 3;
  c.r = c.lane & 31; c.h = c.lane >> 5;
  const int xcd = blockIdx.x & 7, j = blockIdx.x >> 3;
  const int rb1 = (xcd >> 1) * 4 + (j & 3), cb1 = (xcd & 1) * 8 + (j >> 2);
  const int rb3 = xcd * 4 + (j & 3), cb3 = j >> 2, rb4 = xcd * 2 + (j & 1), cb4 = j >> 1;
  c.tag = 0;
  if (c.w8 >= 4) {
    STAMP(0, 0);
    p1_load(c, x, wqkv, wo, w1, w2, rb1, cb1);
  } else {
    if (threadIdx.x < EV_COUNT * 4) *(LAS unsigned*)(c.lds + L_CNT + threadIdx.x * 4) = 0;
    if (c.w8 == 3) {
      __builtin_amdgcn_s_sleep(4);
      if (j < 5) touch_lds(c, c.rws, c.lane * 65536, j * (64u * 65536u));
      else if (j == 5) touch_lds(c, mkrs(out, S_ * D_ * 2), c.lane * 65536, 0);
      else if (j < 9) touch_lds(c, mkrs(j == 6 ? wo : (j == 7 ? w1 : w2), j == 6 ? D_ * D_ * 2 : D_ * DFF * 2), c.lane * 65536, 0);
      else if (j < 15) touch_lds(c, mkrs((const void*)(pc & ~255ull), CODE_BYTES), c.lane * 128, (j - 9) * 8192u);
    }
  }
  c.tag = __builtin_amdgcn_readfirstlane(__builtin_amdgcn_raw_buffer_load_b32(c.rws, 0, OFF_EP, 16)) * 0x9E3779B1u + 0x7F4A7C15u;
  if (c.w8 < 4) {
    if (cb1 < 10) p1_compute<3>(c, rb1, cb1);
    else if (cb1 == 10) p1_compute<2>(c, rb1, cb1);
    else p1_compute<0>(c, rb1, cb1);
  }
  u32x2 xr[4];
  {
    const rsrc_t rx = mkrs(x, S_ * D_ * 2);
    const unsigned vx = (unsigned)c.r * 896u + c.h * 8, sx = (unsigned)(rb3 * 2 + (c.w & 1)) * (32u * 896u) + (cb3 * 2 + (c.w >> 1)) * 64u;
#pragma unroll
    for (int g = 0; g < 4; ++g) xr[g] = __builtin_amdgcn_raw_buffer_load_b64(rx, vx + sx + g * 16, 0, 0);
  }
  u32x4 bw[16][2];
  p2_attn(c, wo, w1, w2, cb3, cb4, bw);
  if (j >= 28) {
    p2_publish(c);
    if (blockIdx.x == 255 && c.w8 == 0) {
#pragma unroll 1
      for (int i = 0; i < 4; ++i) poll(c, flag_off(1, i * 64 + c.lane));
      if (c.lane == 0) __builtin_amdgcn_raw_buffer_store_b32(c.tag, c.rws, 0, OFF_EP, 16);
    }
    return;
  }
  if (c.w8 >= 4) p345_load(c, w1, w2, cb3, cb4);
  else p345_compute(c, xr, bw, out, rb3, cb3, rb4, cb4);
}

extern "C" void kernel_launch(void* const* d_in, const int* in_sizes, int n_in, void* d_out, int out_size,
                              void* d_ws, size_t ws_size, hipStream_t stream) {
  (void)hipFuncSetAttribute((const void*)block_fwd, hipFuncAttributeMaxDynamicSharedMemorySize, LDS_BYTES);
  block_fwd<<<256, 512, LDS_BYTES, stream>>>((const char*)d_in[0], (const char*)d_in[1], (const char*)d_in[2],
                                             (const char*)d_in[4], (const char*)d_in[5], (char*)d_ws, (char*)d_out);
}
```

```cpp
#include <hip/hip_runtime.h>
#include <hip/hip_bf16.h>
#include <stdint.h>

typedef __attribute__((ext_vector_type(8))) short bf16x8;
typedef __attribute__((ext_vector_type(4))) short s16x4;
typedef __attribute__((ext_vector_type(2))) short s16x2;
typedef __attribute__((ext_vector_type(2))) __bf16 bf16x2;
typedef __attribute__((ext_vector_type(16))) float f32x16;
typedef __attribute__((ext_vector_type(8))) float f32x8;
typedef __attribute__((ext_vector_type(4))) float f32x4;
typedef __attribute__((ext_vector_type(2))) float f32x2;
typedef __attribute__((ext_vector_type(4))) unsigned u32x4;
typedef __attribute__((ext_vector_type(2))) unsigned u32x2;
typedef __amdgpu_buffer_rsrc_t rsrc_t;
#define LAS __attribute__((address_space(3)))
typedef LAS char* lptr;

constexpr int S_ = 2048, D_ = 448, DFF = 1792;
constexpr unsigned QKV_BYTES = 16u * 64u * 2048u;
constexpr unsigned OFF_Q = 0, OFF_K = QKV_BYTES, OFF_V = 2 * QKV_BYTES, OFF_O = 3 * QKV_BYTES;
constexpr unsigned OFF_H = OFF_O + 64u * 32u * 1024u;
constexpr unsigned OFF_G = OFF_H + 64u * 28u * 1024u;
constexpr unsigned OFF_ST = OFF_G + 64u * 112u * 1024u;
constexpr unsigned OFF_FL = OFF_ST + 14u * 2048u * 8u;
constexpr unsigned OFF_EP = OFF_FL + 4u * 256u * 256u;
constexpr unsigned OFF_LN = OFF_EP + 256u;
constexpr unsigned WS_USED = OFF_LN + 2048u * 16u;
constexpr unsigned OOB = 0x7F000000u;
constexpr int SLOT = 16384, L_XA = 3 * SLOT, L_WO = 6 * SLOT, L_ML = L_WO + 57 * 1024, L_XB = 35 * 4096, LDS_BYTES = 163840;
constexpr int L_CS = L_ML + 4096, L_ROW = L_CS + 1536, L_CNT = LDS_BYTES - 1024, L_DUMMY = L_CNT + 512;
constexpr int P1_SLOT = 16384 + 12288;
enum { EV_PUB1, EV_PUB2, EV_PUB3, EV_PUB4, EV_GO2, EV_GO3, EV_GO4, EV_GO5, EV_XA, EV_WO, EV_BPRE, EV_W1, EV_CS,
       EV_RED3, EV_A4, EV_X4, EV_W2A, EV_SEG0, EV_SEG1, EV_SEG2, EV_W2B, EV_RED5, EV_COUNT };

constexpr unsigned CODE_BYTES = 42 * 1024;
constexpr int PACE = 2;
#define W5 14
constexpr float QSCALE = 0.18898223650461362f * 1.4426950408889634f;
constexpr float EPS = 1e-5f;
constexpr unsigned ONES2 = 0x3F803F80u;

#define MFMA(a, b, c) __builtin_amdgcn_mfma_f32_32x32x16_bf16((a), (b), (c), 0, 0, 0)
#define WAITV_BAR(N) asm volatile("s_waitcnt vmcnt(" #N ")\n\ts_barrier" ::: "memory")
#define BAR() asm volatile("s_waitcnt lgkmcnt(0)\n\ts_barrier" ::: "memory")
#define SBAR() __builtin_amdgcn_sched_barrier(0)
#define PIN(x) asm volatile("" : "+v"(x))
#define EX(v) __builtin_amdgcn_exp2f(v)
#define STAMP(kid, i) do {} while (0)

__device__ __forceinline__ rsrc_t mkrs(const void* p, unsigned bytes) {
  return __builtin_amdgcn_make_buffer_rsrc((void*)p, (short)0, (int)bytes, 0x00020000);
}
__device__ __forceinline__ void dma16(rsrc_t r, unsigned voff, unsigned soff, unsigned ldsaddr) {
  asm volatile("s_mov_b32 m0, %0\n\ts_nop 0\n\tbuffer_load_dwordx4 %1, %2, %3 offen lds"
               :: "s"(ldsaddr), "v"(voff), "s"(r), "s"(soff) : "memory");
}
__device__ __forceinline__ void dma16x4_sc1(rsrc_t r, unsigned voff, unsigned soff, unsigned ldsaddr) {
  asm volatile("s_mov_b32 m0, %0\n\ts_nop 0\n\tbuffer_load_dwordx4 %1, %2, %3 offen sc1 lds\n\t"
               "buffer_load_dwordx4 %1, %2, %3 offen offset:1024 sc1 lds\n\t"
               "buffer_load_dwordx4 %1, %2, %3 offen offset:2048 sc1 lds\n\t"
               "buffer_load_dwordx4 %1, %2, %3 offen offset:3072 sc1 lds"
               :: "s"(ldsaddr), "v"(voff), "s"(r), "s"(soff) : "memory");
}
template <int IMM>
__device__ __forceinline__ void ld16(u32x4& d, rsrc_t r, unsigned voff, unsigned soff) {
  asm volatile("buffer_load_dwordx4 %0, %1, %2, %3 offen offset:%4 sc1" : "=v"(d) : "v"(voff), "s"(r), "s"(soff), "i"(IMM) : "memory");
}
__device__ __forceinline__ void ld16p(u32x4& d, rsrc_t r, unsigned voff, unsigned soff) {
  asm volatile("buffer_load_dwordx4 %0, %1, %2, %3 offen" : "=v"(d) : "v"(voff), "s"(r), "s"(soff) : "memory");
}
template <int N>
__device__ __forceinline__ void waitv(u32x4& x) { asm volatile("s_waitcnt vmcnt(%1)" : "+v"(x) : "i"(N) : "memory"); }
template <int IMM>
__device__ __forceinline__ void st16(u32x4 v, rsrc_t r, unsigned voff, unsigned soff) {
  asm volatile("buffer_store_dwordx4 %0, %1, %2, %3 offen offset:%4 sc1\n\ts_nop 1" :: "v"(v), "v"(voff), "s"(r), "s"(soff), "i"(IMM) : "memory");
}
__device__ __forceinline__ void st16s(int s, u32x4 v, rsrc_t r, unsigned voff, unsigned soff) {
  if (s) st16<1024>(v, r, voff, soff); else st16<0>(v, r, voff, soff);
}
__device__ __forceinline__ unsigned pk2(float a, float b) {
  f32x2 v = {a, b};
  return __builtin_bit_cast(unsigned, __builtin_convertvector(v, bf16x2));
}
__device__ __forceinline__ float bflo(unsigned u) { return __uint_as_float(u << 16); }
__device__ __forceinline__ float bfhi(unsigned u) { return __uint_as_float(u & 0xffff0000u); }
__device__ __forceinline__ float dot2(unsigned a, unsigned b, float c) {
  return __builtin_amdgcn_fdot2_f32_bf16(__builtin_bit_cast(bf16x2, a), __builtin_bit_cast(bf16x2, b), c, false);
}
__device__ __forceinline__ u32x4 lds128(lptr p) { return *(LAS u32x4*)p; }
__device__ __forceinline__ f32x4 ldsf4(lptr p) { return *(LAS f32x4*)p; }
__device__ __forceinline__ u32x2 ldstr(lptr p) {
  return __builtin_bit_cast(u32x2, __builtin_amdgcn_ds_read_tr16_b64_v4i16((LAS s16x4*)p));
}
__device__ __forceinline__ u32x4 ldstr2(lptr p, int d) {
  const u32x2 a = ldstr(p), b = ldstr(p + d);
  return (u32x4){a[0], a[1], b[0], b[1]};
}
__device__ __forceinline__ bf16x8 frag(u32x4 v) { return __builtin_bit_cast(bf16x8, v); }
__device__ __forceinline__ float xhalf_sum(float v) {
  auto r = __builtin_amdgcn_permlane32_swap(__float_as_uint(v), __float_as_uint(v), false, false);
  return __uint_as_float(r[0]) + __uint_as_float(r[1]);
}
__device__ __forceinline__ float xhalf_max(float v) {
  auto r = __builtin_amdgcn_permlane32_swap(__float_as_uint(v), __float_as_uint(v), false, false);
  return fmaxf(__uint_as_float(r[0]), __uint_as_float(r[1]));
}
__device__ __forceinline__ u32x4 colfrag(float cs, int h) {
  const unsigned a = pk2(cs, cs);
  const float lo = cs - bflo(a);
  const unsigned b = pk2(lo, lo);
  return (u32x4){h ? 0u : a, h ? 0u : b, 0u, 0u};
}
__device__ __forceinline__ u32x4 rowfrag(float nmu) {
  const unsigned a = pk2(nmu, nmu);
  const unsigned p = pk2(nmu, nmu - bflo(a));
  return (u32x4){p, p, 0u, 0u};
}

__device__ __forceinline__ void pin16(f32x16& x) { PIN(x); }
__device__ __forceinline__ void pin4(u32x4& x) { PIN(x); }
__device__ __forceinline__ void pinf(float& x) { PIN(x); }
template <int V> struct IC { static constexpr int value = V; };
template <int I, int N, class F>
__device__ __forceinline__ void static_for(F&& f) {
  if constexpr (I < N) { f(IC<I>{}); static_for<I + 1, N>(f); }
}

struct Ctx {
  lptr lds; unsigned lbase;
  char* ws;
  rsrc_t rws;
  unsigned tag;
  rsrc_t rwarm; unsigned vwarm, swarm;
  int lane, w8, w, r, h;
};

__device__ __forceinline__ void stampf(const Ctx& c, int kid, int i) { STAMP(kid, i); }
__device__ __forceinline__ void ev_set(const Ctx& c, int idx, int word) {
  const unsigned addr = c.lbase + L_CNT + idx * 16 + word * 4, one = 1;
  asm volatile("s_mov_b64 exec, 1\n\tds_write_b32 %0, %1\n\ts_mov_b64 exec, -1" :: "v"(addr), "v"(one) : "memory");
}
template <int NWORDS, int SLEEP = 0>
__device__ __forceinline__ void ev_wait(const Ctx& c, int idx) {
  const unsigned addr = c.lbase + L_CNT + idx * 16;
#pragma unroll 1
  for (int spins = 0; spins < (1 << 24); ++spins) {
    u32x4 v;
    asm volatile("ds_read_b128 %0, %1\n\ts_waitcnt lgkmcnt(0)" : "=v"(v) : "v"(addr) : "memory");
    if (__builtin_amdgcn_readfirstlane(NWORDS == 4 ? (v[0] & v[1] & v[2] & v[3]) : v[0])) break;
    if (SLEEP) __builtin_amdgcn_s_sleep(SLEEP);
  }
}
template <int SLEEP = 0>
__device__ __forceinline__ void ev_wait_half(const Ctx& c, int idx) {
  const unsigned addr = c.lbase + L_CNT + idx * 16 + (c.w >> 1) * 8;
#pragma unroll 1
  for (int spins = 0; spins < (1 << 24); ++spins) {
    u32x2 v;
    asm volatile("ds_read_b64 %0, %1\n\ts_waitcnt lgkmcnt(0)" : "=v"(v) : "v"(addr) : "memory");
    if (__builtin_amdgcn_readfirstlane(v[0] & v[1])) break;
    if (SLEEP) __builtin_amdgcn_s_sleep(SLEEP);
  }
}
template <int NW>
__device__ __forceinline__ void publish(const Ctx& c, int idx, unsigned flag_off) {
  const unsigned addr = c.lbase + L_CNT + idx * 16, one = 1;
  unsigned old;
  asm volatile("s_waitcnt vmcnt(0)\n\ts_mov_b64 exec, 1\n\tds_add_rtn_u32 %0, %1, %2\n\ts_mov_b64 exec, -1\n\ts_waitcnt lgkmcnt(0)"
               : "=v"(old) : "v"(addr), "v"(one) : "memory");
  if (__builtin_amdgcn_readfirstlane(old) == NW - 1) {
    const unsigned tag = c.tag;
    asm volatile("s_mov_b64 exec, 1\n\tbuffer_store_dword %0, off, %1, %2 sc1\n\ts_mov_b64 exec, -1" :: "v"(tag), "s"(c.rws), "s"(flag_off) : "memory");
  }
}
__device__ __forceinline__ void poll(const Ctx& c, unsigned voff) {
#pragma unroll 1
  for (int spins = 0; spins < (1 << 17); ++spins) {
    unsigned f;
    asm volatile("buffer_load_dword %0, %1, %2, 0 offen sc1\n\ts_waitcnt vmcnt(0)" : "=v"(f) : "v"(voff), "s"(c.rws) : "memory");
    if (__all(f == c.tag)) break;
  }
}
__device__ __forceinline__ void acquire(const Ctx& c, unsigned voff, int ev) {
  if (c.w == 0) { poll(c, voff); ev_set(c, ev, 0); }
  else ev_wait<1>(c, ev);
}
__device__ __forceinline__ unsigned flag_off(int phase, int idx) { return OFF_FL + (unsigned)(phase * 256 + idx) * 256u; }
__device__ __forceinline__ void touch_lds(const Ctx& c, rsrc_t r, unsigned voff, unsigned soff) {
  asm volatile("s_mov_b32 m0, %0\n\ts_nop 0\n\tbuffer_load_dword %1, %2, %3 offen lds" :: "s"(c.lbase + L_DUMMY), "v"(voff), "s"(r), "s"(soff) : "memory");
}

template <int CTRL>
__device__ __forceinline__ float dpp_add(float v) {
  return v + __int_as_float(__builtin_amdgcn_update_dpp(0, __float_as_int(v), CTRL, 0xf, 0xf, true));
}
__device__ __forceinline__ float half_sum(float v) {
  v = dpp_add<0x140>(dpp_add<0x141>(dpp_add<0x4E>(dpp_add<0xB1>(v))));
  auto r = __builtin_amdgcn_permlane16_swap(__float_as_uint(v), __float_as_uint(v), false, false);
  return __uint_as_float(r[0]) + __uint_as_float(r[1]);
}

template <int NQ>
__device__ __forceinline__ void p1_compute(Ctx& c, const char* x, int rb, int cb) {
  const int lane = c.lane, w = c.w, r = c.r, h = c.h;
  lptr lds = c.lds;
  const int srow_g = rb * 128 + cb * 8 + w * 2 + h;
  u32x4 xs0, xs1;
  {
    const rsrc_t rx = mkrs(x, S_ * D_ * 2);
    const unsigned vo = r < 28 ? (unsigned)srow_g * 896u + r * 32u : OOB;
    ld16p(xs0, rx, vo, 0); ld16p(xs1, rx, vo, 16);
    __builtin_amdgcn_s_sleep(6);
    touch_lds(c, c.rwarm, c.vwarm, c.swarm);
  }
  const int arow = w * 32 + r;
  const unsigned aoff = arow * 128 + ((h ^ ((arow >> 1) & 7)) * 16);
  const unsigned boff = 16384 + (8 * h + ((lane & 15) >> 2)) * 192 + ((lane >> 4) & 1) * 32 + (lane & 3) * 8 + (cb & 1) * 8;
  unsigned ao[4], bo[4];
#pragma unroll
  for (int i = 0; i < 4; ++i) { const int ks = (w + i) & 3; ao[i] = aoff ^ (ks * 32); bo[i] = boff + ks * 3072; }
  f32x16 acc[3] = {};
  float cs[3] = {0.f, 0.f, 0.f};
  u32x4 a[4], b[4][3];
  auto dots = [&]() {
#pragma unroll
    for (int e = 0; e < 4; ++e)
#pragma unroll
      for (int n = 0; n < 3; ++n) cs[n] = dot2(b[0][n][e], ONES2, cs[n]);
    PIN(cs[0]); PIN(cs[1]); PIN(cs[2]);
    SBAR();
  };
  {
    unsigned prev;
    asm volatile("s_load_dword %0, %1, 0x0\n\ts_waitcnt lgkmcnt(0)\n\ts_barrier" : "=s"(prev) : "s"(c.ws + OFF_EP) : "memory");
    c.tag = prev * 0x9E3779B1u + 0x7F4A7C15u;
  }
  STAMP(0, 1);
#pragma unroll
  for (int i = 0; i < 4; ++i) {
#pragma unroll
    for (int n = 0; n < 3; ++n) b[i][n] = ldstr2(lds + bo[i] + n * 56, 768);
    a[i] = lds128(lds + ao[i]);
  }
  SBAR();
  {
    asm volatile("s_waitcnt vmcnt(1)" : "+v"(xs0), "+v"(xs1) :: "memory");
    float s0 = 0.f, s1 = 0.f, q0 = 0.f, q1 = 0.f;
#pragma unroll
    for (int e = 0; e < 4; ++e) { s0 = dot2(xs0[e], ONES2, s0); s1 = dot2(xs1[e], ONES2, s1); q0 = dot2(xs0[e], xs0[e], q0); q1 = dot2(xs1[e], xs1[e], q1); }
    const float mu = half_sum(s0 + s1) * (1.f / 448.f);
    const float rstd = __builtin_amdgcn_rsqf(fmaxf(half_sum(q0 + q1) * (1.f / 448.f) - mu * mu, 0.f) + EPS);
    st16<0>((u32x4){__float_as_uint(-mu), c.tag, __float_as_uint(rstd), c.tag}, c.rws, r == 0 ? (unsigned)srow_g * 16u : OOB, OFF_LN);
    SBAR();
  }
  u32x4 ln = {};
  const unsigned vln = (unsigned)(rb * 128 + arow) * 16u;
#pragma unroll 1
  for (int st = 1; st < 7; ++st) {
    lptr sb = lds + (st >= 5 ? st - 5 : st) * P1_SLOT;
    dots();
    asm volatile("s_barrier" ::: "memory");
#pragma unroll
    for (int i = 0; i < 4; ++i)
#pragma unroll
      for (int n = 0; n < 3; ++n) {
        acc[n] = n < NQ ? MFMA(frag(b[i][n]), frag(a[i]), acc[n]) : MFMA(frag(a[i]), frag(b[i][n]), acc[n]);
        SBAR();
        b[i][n] = ldstr2(sb + bo[i] + n * 56, 768);
        if (n == 2) a[i] = lds128(sb + ao[i]);
        if (i == 0 && n == 0) asm volatile("buffer_load_dwordx4 %0, %1, %2, %3 offen sc1" : "+v"(ln) : "v"(vln), "s"(c.rws), "s"(st == 6 ? OFF_LN : OOB) : "memory");
        SBAR();
      }
  }
  STAMP(0, 7);
  dots();
#pragma unroll
  for (int i = 0; i < 2; ++i)
#pragma unroll
    for (int n = 0; n < 3; ++n) {
      acc[n] = n < NQ ? MFMA(frag(b[i][n]), frag(a[i]), acc[n]) : MFMA(frag(a[i]), frag(b[i][n]), acc[n]);
      if (i == 0) {
        const float t = xhalf_sum(cs[n]);
        *(LAS float*)(lds + L_CS + (n * 32 + r) * 16 + w * 4) = t;
      }
      SBAR();
    }
  BAR();
  f32x4 p4[3], r4[4];
  u32x4 cf[3];
#pragma unroll
  for (int n = 0; n < 3; ++n) p4[n] = ldsf4(lds + L_CS + (n * 32 + r) * 16);
  SBAR();
#pragma unroll
  for (int i = 2; i < 4; ++i)
#pragma unroll
    for (int n = 0; n < 3; ++n) {
      acc[n] = n < NQ ? MFMA(frag(b[i][n]), frag(a[i]), acc[n]) : MFMA(frag(a[i]), frag(b[i][n]), acc[n]);
      if (i == 3) {
        float t0 = p4[n][0] + p4[n][1], t1 = p4[n][2] + p4[n][3];
        PIN(t0); PIN(t1);
        cf[n] = colfrag(t0 + t1, h);
        PIN(cf[n]);
      }
      SBAR();
    }
  pin16(acc[0]); pin16(acc[1]); pin16(acc[2]);
  waitv<0>(ln);
#pragma unroll 1
  for (int spins = 0; spins < (1 << 17) && !__all(ln[1] == c.tag && ln[3] == c.tag); ++spins)
    asm volatile("buffer_load_dwordx4 %0, %1, %2, %3 offen sc1\n\ts_waitcnt vmcnt(0)" : "=v"(ln) : "v"(vln), "s"(c.rws), "s"(OFF_LN) : "memory");
  const float rstd = __uint_as_float(ln[2]);
  const u32x4 rf = rowfrag(__uint_as_float(ln[0]));
  if (NQ < 3) {
    lptr srow = lds + L_ROW + w * 128;
    *(LAS float*)(srow + r * 4) = rstd;
#pragma unroll
    for (int g = 0; g < 4; ++g) r4[g] = ldsf4(srow + (8 * g + 4 * h) * 4);
  }
#pragma unroll
  for (int n = 0; n < 3; ++n) acc[n] = n < NQ ? MFMA(frag(cf[n]), frag(rf), acc[n]) : MFMA(frag(rf), frag(cf[n]), acc[n]);
  STAMP(0, 8);
  const int t = rb * 4 + w;
#pragma unroll
  for (int n = 0; n < 3; ++n) {
    const int u = cb * 3 + n;
    const float sc = u < 16 ? rstd * QSCALE : rstd;
#pragma unroll
    for (int s = 0; s < 2; ++s) {
      u32x4 o;
#pragma unroll
      for (int i = 0; i < 4; ++i) {
        const int e = 8 * s + 2 * i;
        o[i] = n < NQ ? pk2(acc[n][e] * sc, acc[n][e + 1] * sc) : pk2(acc[n][e] * r4[e >> 2][e & 3], acc[n][e + 1] * r4[e >> 2][(e & 3) + 1]);
      }
      if (n < NQ) {
        if (s == 1 && h) { o[2] = u < 16 ? 0u : ONES2; o[3] = u < 16 ? 0u : 0x00003F80u; }
      } else if (r >= 28) {
        o = (u32x4){ONES2, ONES2, ONES2, ONES2};
      }
      st16s(s, o, c.rws, lane * 16, (unsigned)(u * 64 + t) * 2048u);
    }
  }
  STAMP(0, 9);
}

__device__ __forceinline__ void p1_load(const Ctx& c, const char* x, const char* wqkv, const char* wo, const char* w1, const char* w2, int rb, int cb) {
  const int lane = c.lane, w = c.w;
  const rsrc_t rx = mkrs(x, S_ * D_ * 2), rw = mkrs(wqkv, D_ * 3 * D_ * 2);
  unsigned vxa[2], vw[3];
#pragma unroll
  for (int p = 0; p < 2; ++p) vxa[p] = (unsigned)(lane >> 3) * 896u + (unsigned)((lane & 7) ^ ((4 * p + (lane >> 4)) & 7)) * 16u;
#pragma unroll
  for (int q = 0; q < 3; ++q) { const int i = q * 64 + lane; vw[q] = (unsigned)(i / 12) * 2688u + (unsigned)(i % 12) * 16u; }
  const unsigned sx0 = (unsigned)(rb * 128 + w * 32) * 896u, sw0 = (((unsigned)cb * 168u) & ~15u) + (unsigned)w * (16u * 2688u);
  auto issue = [&](int st) {
    const unsigned lb = c.lbase + (st >= 5 ? st - 5 : st) * P1_SLOT;
#pragma unroll
    for (int p = 0; p < 4; ++p) dma16(rx, vxa[p & 1], sx0 + st * 128u + p * 7168u, lb + (w * 4 + p) * 1024);
#pragma unroll
    for (int q = 0; q < 3; ++q) dma16(rw, vw[q], sw0 + st * (64u * 2688u), lb + 16384 + (w * 3 + q) * 1024);
  };
  issue(0);
  touch_lds(c, rw, lane < 10 ? lane * 128 : OOB, (blockIdx.x * 4 + w) * 1176u);
  issue(1);
  WAITV_BAR(8);
#pragma unroll 1
  for (int st = 2; st < 7; ++st) {
    issue(st);
    if (st < 6) WAITV_BAR(7);
  }
  const rsrc_t rt = mkrs(w == 0 ? wo : (w == 1 ? w1 : w2), w == 0 ? D_ * D_ * 2 : (w < 3 ? D_ * DFF * 2 : 0));
  touch_lds(c, rt, lane * 128, blockIdx.x * (w == 0 ? 8192u : 6272u));
  WAITV_BAR(8);
  WAITV_BAR(1);
  BAR();
}

#define ATT_UNIT(SC, SN, KN0, KN1, QN, OPREV, VPREV1, OCUR, VCUR0, HOOK_A, HOOK_B, HOOK_C, HOOK_D)       \
  do {                                                                                                     \
    SN = MFMA(frag(KN0), frag(QN[0]), zero); SBAR();                                                       \
    p1[2] = pk2(e[4], e[5]); p1[3] = pk2(e[6], e[7]); PIN(p1); SBAR();                                     \
    e[0] = EX(SC[0]); e[1] = EX(SC[1]); e[2] = EX(SC[2]); e[3] = EX(SC[3]);                                \
    PIN(e); SBAR(); HOOK_A;                                                                                \
    OPREV = MFMA(frag(VPREV1), frag(p1), OPREV); SBAR();                                                   \
    p0[0] = pk2(e[0], e[1]); p0[1] = pk2(e[2], e[3]); PIN(p0); SBAR();                                     \
    e[4] = EX(SC[4]); e[5] = EX(SC[5]); e[6] = EX(SC[6]); e[7] = EX(SC[7]);                                \
    PIN(e); SBAR(); HOOK_B;                                                                                \
    SN = MFMA(frag(KN1), frag(QN[1]), SN); SBAR();                                                         \
    p0[2] = pk2(e[4], e[5]); p0[3] = pk2(e[6], e[7]); PIN(p0); SBAR();                                     \
    e[0] = EX(SC[8]); e[1] = EX(SC[9]); e[2] = EX(SC[10]); e[3] = EX(SC[11]);                              \
    PIN(e); SBAR(); HOOK_C;                                                                                \
    OCUR = MFMA(frag(VCUR0), frag(p0), OCUR); SBAR();                                                      \
    p1[0] = pk2(e[0], e[1]); p1[1] = pk2(e[2], e[3]); PIN(p1); SBAR();                                     \
    e[4] = EX(SC[12]); e[5] = EX(SC[13]); e[6] = EX(SC[14]); e[7] = EX(SC[15]);                            \
    PIN(e); SBAR(); HOOK_D;                                                                                \
  } while (0)

__device__ __forceinline__ int p4_rot(int cb4) { return cb4 >= 7 ? cb4 - 7 : cb4; }
#define P5ROT (cb3 & 1) * 7
__device__ __forceinline__ int p5_rot(int cb3) { return P5ROT; }
__device__ __forceinline__ unsigned w1_piece(int p, int cb4) {
  const int q = p + p4_rot(cb4) * 16;
  return (unsigned)(q >= 112 ? q - 112 : q) * 14336u + cb4 * 256u;
}
template <bool EXACT>
__device__ __forceinline__ void attn_pass(const Ctx& c, f32x16 (&o)[4], float (&m)[4]) {
  const int lane = c.lane, w = c.w, h = c.h;
  const int hd = (blockIdx.x & 7) * 2 + ((blockIdx.x >> 3) & 1), rb = blockIdx.x >> 4;
  const unsigned vo = lane * 16;
  const unsigned sq = OFF_Q + (unsigned)(hd * 64 + rb * 4) * 2048u;
  const unsigned sk = OFF_K + (unsigned)(hd * 64 + w * 16) * 2048u, sv = sk + QKV_BYTES;
  const unsigned rot = (unsigned)(rb & 7) * 4096u, rot1 = (rot + 4096u) & 32767u;
  const rsrc_t rs = c.rws;
  const f32x16 zero = {};
  u32x4 q0[2], q1[2], q2[2], q3[2], ka0, ka1, va0, va1, kb0, kb1, vb0, vb1, kc0, kc1, vc0, vc1, kd0, kd1, vd0, vd1;
  unsigned sqb[4];
#pragma unroll
  for (int b = 0; b < 4; ++b) { m[b] = -INFINITY; sqb[b] = sq + ((w + 1 + b) & 3) * 2048u; }
#pragma unroll 1
  for (int i = EXACT ? 15 : 0; i >= 0; --i) {
    const unsigned ko = sk + ((rot + i * 2048u) & 32767u);
    ld16<0>(ka0, rs, vo, ko); ld16<0>(q0[0], rs, vo, sqb[0]); ld16<1024>(ka1, rs, vo, ko); ld16<1024>(q0[1], rs, vo, sqb[0]);
    ld16<0>(q1[0], rs, vo, sqb[1]); ld16<1024>(q1[1], rs, vo, sqb[1]); ld16<0>(q2[0], rs, vo, sqb[2]); ld16<1024>(q2[1], rs, vo, sqb[2]);
    ld16<0>(q3[0], rs, vo, sqb[3]); ld16<1024>(q3[1], rs, vo, sqb[3]);
    ld16<0>(va0, rs, vo, sv + rot);
    ld16<1024>(va1, rs, vo, sv + rot); ld16<2048>(kb0, rs, vo, sk + rot); ld16<3072>(kb1, rs, vo, sk + rot); ld16<2048>(vb0, rs, vo, sv + rot);
    asm volatile("s_waitcnt vmcnt(5)" : "+v"(q0[0]), "+v"(q0[1]), "+v"(q1[0]), "+v"(q1[1]), "+v"(q2[0]), "+v"(q2[1]), "+v"(q3[0]), "+v"(q3[1]),
                 "+v"(ka0), "+v"(ka1) :: "memory");
    f32x16 s0 = MFMA(frag(ka0), frag(q0[0]), zero), s1 = MFMA(frag(ka0), frag(q1[0]), zero);
    s0 = MFMA(frag(ka1), frag(q0[1]), s0); s1 = MFMA(frag(ka1), frag(q1[1]), s1);
    f32x16 s2 = MFMA(frag(ka0), frag(q2[0]), zero), s3 = MFMA(frag(ka0), frag(q3[0]), zero);
    s2 = MFMA(frag(ka1), frag(q2[1]), s2); s3 = MFMA(frag(ka1), frag(q3[1]), s3);
    ld16<3072>(vb1, rs, vo, sv + rot); ld16<0>(kc0, rs, vo, sk + rot1); ld16<1024>(kc1, rs, vo, sk + rot1); ld16<0>(vc0, rs, vo, sv + rot1);
    ld16<1024>(vc1, rs, vo, sv + rot1); ld16<2048>(kd0, rs, vo, sk + rot1); ld16<3072>(kd1, rs, vo, sk + rot1); ld16<2048>(vd0, rs, vo, sv + rot1);
#pragma unroll
    for (int e = 0; e < 16; ++e) { m[0] = fmaxf(m[0], s0[e]); m[1] = fmaxf(m[1], s1[e]); m[2] = fmaxf(m[2], s2[e]); m[3] = fmaxf(m[3], s3[e]); }
  }
#pragma unroll
  for (int b = 0; b < 4; ++b) {
    m[b] = xhalf_max(m[b]);
    const float nm = -m[b];
    const unsigned u0 = __float_as_uint(nm) & 0xffff0000u;
    const float r1 = nm - __uint_as_float(u0);
    const unsigned u1 = __float_as_uint(r1) & 0xffff0000u;
    const float r2 = r1 - __uint_as_float(u1);
    u32x4& qq = b == 0 ? q0[1] : (b == 1 ? q1[1] : (b == 2 ? q2[1] : q3[1]));
    qq[2] = h ? ((u0 >> 16) | u1) : qq[2];
    qq[3] = h ? (__float_as_uint(r2) >> 16) : qq[3];
    o[b] = zero;
  }
  STAMP(1, 1);
  f32x16 sa = MFMA(frag(ka0), frag(q0[0]), zero), sb;
  sa = MFMA(frag(ka1), frag(q0[1]), sa);
  u32x4 p0 = {}, p1 = {};
  f32x8 e = {};
  vd1 = (u32x4){};
  unsigned oc = rot;
#define ATT_TILE(K0, K1, V0, V1, KN0, KN1, VP1, VP1_OFF, VP1_S, OFF, KS, VS)                                              \
    ATT_UNIT(sa, sb, K0, K1, q1, o[3], VP1, o[0], V0, (void)0, ld16<VP1_OFF>(VP1, rs, vo, VP1_S),                         \
             asm volatile("s_waitcnt vmcnt(12)" : "+v"(V0), "+v"(V1) :: "memory"), (void)0);                              \
    ATT_UNIT(sb, sa, K0, K1, q2, o[0], V1, o[1], V0, (void)0, (void)0, (void)0, (void)0);                                 \
    ATT_UNIT(sa, sb, K0, K1, q3, o[1], V1, o[2], V0, ld16<OFF>(K0, rs, vo, KS), (void)0, ld16<OFF + 1024>(K1, rs, vo, KS), \
             asm volatile("s_waitcnt vmcnt(12)" : "+v"(KN0), "+v"(KN1) :: "memory"));                                     \
    ATT_UNIT(sb, sa, KN0, KN1, q0, o[2], V1, o[3], V0, (void)0, (void)0, (void)0, ld16<OFF>(V0, rs, vo, VS))
#pragma unroll 1
  for (int i = 0; i < 4; ++i) {
    const unsigned c1 = (oc + 4096u) & 32767u, n0 = (oc + 8192u) & 32767u, n1 = (oc + 12288u) & 32767u;
    const unsigned k0 = i < 3 ? sk + n0 : OOB, k1 = i < 3 ? sk + n1 : OOB, v0 = i < 3 ? sv + n0 : OOB, v1 = i < 3 ? sv + n1 : OOB;
    ATT_TILE(ka0, ka1, va0, va1, kb0, kb1, vd1, 3072, sv + c1, 0, k0, v0);
    ATT_TILE(kb0, kb1, vb0, vb1, kc0, kc1, va1, 1024, v0, 2048, k0, v0);
    ATT_TILE(kc0, kc1, vc0, vc1, kd0, kd1, vb1, 3072, v0, 0, k1, v1);
    ATT_TILE(kd0, kd1, vd0, vd1, ka0, ka1, vc1, 1024, v1, 2048, k1, v1);
    oc = n0;
  }
  p1[2] = pk2(e[4], e[5]); p1[3] = pk2(e[6], e[7]);
  o[3] = MFMA(frag(vd1), frag(p1), o[3]);
  asm volatile("s_waitcnt vmcnt(0)" : "+v"(ka0), "+v"(ka1), "+v"(va0), "+v"(va1), "+v"(kb0), "+v"(kb1), "+v"(vb0), "+v"(vb1),
               "+v"(kc0), "+v"(kc1), "+v"(vc0), "+v"(vc1), "+v"(kd0), "+v"(kd1), "+v"(vd0) :: "memory");
}

__device__ __forceinline__ void p2_attn(const Ctx& c) {
  const int lane = c.lane, w = c.w, r = c.r, h = c.h;
  lptr lds = c.lds;
  const int hd = (blockIdx.x & 7) * 2 + ((blockIdx.x >> 3) & 1), rb = blockIdx.x >> 4;
  {
    const int u = lane < 16 ? 16 + hd : (lane < 32 ? 32 + hd : hd);
    acquire(c, flag_off(0, (u / 3) * 16 + (lane < 32 ? (lane & 15) : rb)), EV_GO2);
  }
  STAMP(1, 0);
  f32x16 o[4];
  float m[4];
  attn_pass<false>(c, o, m);
  bool ok = true;
#pragma unroll
  for (int b = 0; b < 4; ++b) ok &= o[b][12] >= 0x1p-30f && o[b][12] <= 0x1p30f;
  if (!__all(ok || h == 0)) attn_pass<true>(c, o, m);
  STAMP(1, 2);
#pragma unroll
  for (int b = 0; b < 4; ++b) {
    const int blk = (w + 1 + b) & 3;
    if (h) *(LAS f32x2*)(lds + L_ML + blk * 1024 + r * 32 + b * 8) = (f32x2){m[b], o[b][12]};
    if (b < 3) {
      lptr dst = lds + L_XA + (blk * 3 + b) * 4096 + lane * 16;
#pragma unroll
      for (int g = 0; g < 4; ++g) *(LAS f32x4*)(dst + g * 1024) = (f32x4){o[b][4 * g], o[b][4 * g + 1], o[b][4 * g + 2], o[b][4 * g + 3]};
    }
  }
  ev_set(c, EV_XA, w);
  pin16(o[3]);
  ev_wait<4>(c, EV_XA);
  STAMP(1, 4);
  const f32x4 a0 = ldsf4(lds + L_ML + w * 1024 + r * 32), a1 = ldsf4(lds + L_ML + w * 1024 + r * 32 + 16);
  f32x4 v[3][4];
#pragma unroll
  for (int g = 0; g < 4; ++g)
#pragma unroll
    for (int b = 0; b < 3; ++b) v[b][g] = ldsf4(lds + L_XA + (w * 3 + b) * 4096 + lane * 16 + g * 1024);
  const float M = fmaxf(fmaxf(a0[0], a0[2]), fmaxf(a1[0], a1[2]));
  float wk[4] = {EX(a0[0] - M), EX(a0[2] - M), EX(a1[0] - M), EX(a1[2] - M)};
  const float inv = __builtin_amdgcn_rcpf((a0[1] * wk[0] + a0[3] * wk[1]) + (a1[1] * wk[2] + a1[3] * wk[3]));
#pragma unroll
  for (int k = 0; k < 4; ++k) wk[k] *= inv;
  u32x4 res[2];
#pragma unroll
  for (int g = 0; g < 4; ++g) {
    const f32x4 own = {o[3][4 * g], o[3][4 * g + 1], o[3][4 * g + 2], o[3][4 * g + 3]};
    const f32x4 f = (own * wk[3] + v[0][g] * wk[0]) + (v[1][g] * wk[1] + v[2][g] * wk[2]);
    res[g >> 1][(g & 1) * 2] = pk2(f[0], f[1]); res[g >> 1][(g & 1) * 2 + 1] = pk2(f[2], f[3]);
  }
  if (h) { res[1][2] = 0; res[1][3] = 0; }
#pragma unroll
  for (int s = 0; s < 2; ++s) st16s(s, res[s], c.rws, lane * 16, OFF_O + (unsigned)((rb * 4 + w) * 16 + hd) * 2048u);
  STAMP(1, 5);
}
__device__ __forceinline__ void p2_publish(const Ctx& c) {
  publish<4>(c, EV_PUB2, flag_off(1, (blockIdx.x >> 4) * 16 + (blockIdx.x & 7) * 2 + ((blockIdx.x >> 3) & 1)));
}

__device__ __forceinline__ void stats_issue(const Ctx& c, int t, f32x2 (&v)[7]) {
  const unsigned vo = (c.h * 7 * 2048 + c.r) * 8;
#pragma unroll
  for (int i = 0; i < 7; ++i)
    asm volatile("buffer_load_dwordx2 %0, %1, %2, %3 offen sc1" : "=v"(v[i]) : "v"(vo), "s"(c.rws), "s"(OFF_ST + (unsigned)(i * 2048 + t * 32) * 8u) : "memory");
}
#define STATS_WAIT(N, v) asm volatile("s_waitcnt vmcnt(" #N ")" : "+v"(v[0]), "+v"(v[1]), "+v"(v[2]), "+v"(v[3]), "+v"(v[4]), "+v"(v[5]), "+v"(v[6]) :: "memory")
__device__ __forceinline__ void stats_pin(f32x2 (&v)[7]) { asm volatile("" : "+v"(v[0]), "+v"(v[1]), "+v"(v[2]), "+v"(v[3]), "+v"(v[4]), "+v"(v[5]), "+v"(v[6])); }
__device__ __forceinline__ void stats_finish(const f32x2 (&v)[7], float& mu, float& rstd) {
  const f32x2 t = ((v[0] + v[1]) + (v[2] + v[3])) + ((v[4] + v[5]) + v[6]);
  mu = xhalf_sum(t[0]) * (1.f / 448.f);
  rstd = __builtin_amdgcn_rsqf(fmaxf(xhalf_sum(t[1]) * (1.f / 448.f) - mu * mu, 0.f) + EPS);
}
__device__ __forceinline__ unsigned relu2(float a, float b) {
  return __builtin_bit_cast(unsigned, __builtin_elementwise_max(__builtin_bit_cast(s16x2, pk2(a, b)), (s16x2){0, 0}));
}
constexpr int w1_slot(int st) { return st; }
constexpr int w2_pos(int k) { return k < 35 ? k : k - 35; }

__device__ __forceinline__ void pair_reduce(const Ctx& c, f32x16 (&acc)[2], int lbuf, int ev) {
  lptr dst = c.lds + lbuf + c.w * 4096 + c.lane * 16;
#pragma unroll
  for (int g = 0; g < 4; ++g) *(LAS f32x4*)(dst + g * 1024) = (f32x4){acc[1][4 * g], acc[1][4 * g + 1], acc[1][4 * g + 2], acc[1][4 * g + 3]};
  ev_set(c, ev, c.w);
  PIN(acc[0]);
  const unsigned fa = c.lbase + L_CNT + ev * 16 + (c.w ^ 2) * 4, sa = c.lbase + lbuf + (c.w ^ 2) * 4096 + c.lane * 16;
  f32x4 v0, v1, v2, v3;
#pragma unroll 1
  for (int spins = 0; spins < (1 << 24); ++spins) {
    unsigned f;
    asm volatile("ds_read_b32 %0, %5\n\tds_read_b128 %1, %6\n\tds_read_b128 %2, %6 offset:1024\n\tds_read_b128 %3, %6 offset:2048\n\t"
                 "ds_read_b128 %4, %6 offset:3072\n\ts_waitcnt lgkmcnt(0)"
                 : "=&v"(f), "=&v"(v0), "=&v"(v1), "=&v"(v2), "=&v"(v3) : "v"(fa), "v"(sa) : "memory");
    if (__builtin_amdgcn_readfirstlane(f)) break;
  }
#pragma unroll
  for (int e = 0; e < 4; ++e) { acc[0][e] += v0[e]; acc[0][4 + e] += v1[e]; acc[0][8 + e] += v2[e]; acc[0][12 + e] += v3[e]; }
}

__device__ __forceinline__ void p345_compute(const Ctx& c, const u32x2 (&xr)[4], char* out, int rb3, int cb3, int rb4, int cb4) {
  const int lane = c.lane, w = c.w, r = c.r, h = c.h;
  lptr lds = c.lds;
  const rsrc_t rs = c.rws;
  const int wr = w & 1, kh = w >> 1, q = (lane & 15) >> 2;
  const int t3 = rb3 * 2 + wr, sub = cb3 * 2 + kh;
  const unsigned trl = ((lane >> 4) & 1) * 32 + (lane & 3) * 8, va = lane * 16;
  u32x4 hres[2];
  {
    u32x4 bw[16][2];
    ev_wait<4>(c, EV_WO);
    const unsigned b0 = L_WO + (kh * 224 + 4 * h + q) * 128 + trl;
    unsigned bb[2] = {b0 + ((kh ^ (q >> 1)) * 64), b0 + ((kh ^ 1 ^ (q >> 1)) * 64)};
    PIN(bb[0]); PIN(bb[1]);
#pragma unroll
    for (int ks = 0; ks < 16; ++ks) {
      if (ks == 8) { SBAR(); p2_publish(c); STAMP(1, 6); }
#pragma unroll
      for (int i = 0; i < 2; ++i) bw[ks][i] = ldstr2(lds + bb[i] + ((ks >> 1) * 28 + (ks & 1) * 16) * 128, 1024);
    }
    ev_set(c, EV_BPRE, w);
    f32x16 acc[2];
#pragma unroll
    for (int g = 0; g < 4; ++g)
#pragma unroll
      for (int i = 0; i < 2; ++i) { acc[0][4 * g + 2 * i] = bflo(xr[g][i]); acc[0][4 * g + 2 * i + 1] = bfhi(xr[g][i]); }
    acc[1] = f32x16{};
    pin16(acc[0]); pin16(acc[1]);
    ev_wait<4>(c, EV_BPRE);
    acquire(c, flag_off(1, (rb3 >> 1) * 16 + (lane & 15)), EV_GO3);
    STAMP(2, 0);
    const unsigned sa = OFF_O + (unsigned)(t3 * 32 + kh * 16) * 1024u;
    u32x4 a[16];
    static_for<0, 10>([&](auto kc) { constexpr int k = decltype(kc)::value; ld16<(k & 3) * 1024>(a[k], rs, va, sa + (k >> 2) * 4096u); });
    static_for<0, 16>([&](auto kc) {
      constexpr int k = decltype(kc)::value;
      waitv<(k <= 6 ? 9 : 15 - k)>(a[k]);
      acc[1] = MFMA(frag(bw[k][1]), frag(a[k]), acc[1]);
      SBAR();
      if constexpr (k + 10 < 16) ld16<((k + 10) & 3) * 1024>(a[k + 10], rs, va, sa + ((k + 10) >> 2) * 4096u);
      SBAR();
      acc[0] = MFMA(frag(bw[k][0]), frag(a[k]), acc[0]);
      SBAR();
    });
    STAMP(2, 7);
    pair_reduce(c, acc, L_XB, EV_RED3);
    STAMP(2, 8);
#pragma unroll
    for (int i = 0; i < 8; ++i) hres[i >> 2][i & 3] = pk2(acc[0][2 * i], acc[0][2 * i + 1]);
#pragma unroll
    for (int sx = 0; sx < 2; ++sx) st16s(sx, hres[sx], rs, va, OFF_H + (unsigned)(t3 * 28 + sub * 2) * 1024u);
    f32x4 s4 = {}, q4 = {};
#pragma unroll
    for (int g = 0; g < 4; ++g) {
      const f32x4 v = {acc[0][4 * g], acc[0][4 * g + 1], acc[0][4 * g + 2], acc[0][4 * g + 3]};
      s4 += v; q4 += v * v;
    }
    const float s = xhalf_sum((s4[0] + s4[1]) + (s4[2] + s4[3])), sq = xhalf_sum((q4[0] + q4[1]) + (q4[2] + q4[3]));
    if (h == 0) __builtin_amdgcn_raw_buffer_store_b64((u32x2){__float_as_uint(s), __float_as_uint(sq)}, rs, r * 8, OFF_ST + (unsigned)(sub * 2048 + t3 * 32) * 8u, 16);
    STAMP(2, 9);
  }
  {
    const int t4 = rb4 * 4 + w;
    unsigned bo[4][3];
#pragma unroll
    for (int i = 0; i < 4; ++i) {
      bo[i][0] = (4 * h + q) * 256 + ((i ^ q) * 64) + trl; bo[i][1] = bo[i][0] + 65536; bo[i][2] = bo[i][0] + 131072;
      PIN(bo[i][0]); PIN(bo[i][1]); PIN(bo[i][2]);
    }
    ev_wait<4>(c, EV_CS);
    u32x2 cf[4];
#pragma unroll
    for (int i = 0; i < 4; ++i) { const u32x4 t = colfrag(*(LAS float*)(lds + L_CS + i * 128 + r * 4), h); cf[i] = (u32x2){t[0], t[1]}; PIN(cf[i]); }
    u32x4 a[28], bq[4][2];
    auto bread = [&](auto kc) {
      constexpr int k = decltype(kc)::value, pass = k / 28, ks = k % 28, off = w1_slot(ks >> 2) * SLOT + (ks & 3) * 4096;
      bq[k & 3][0] = ldstr2(lds + bo[2 * pass][off >> 16] + (off & 65535), 2048);
      bq[k & 3][1] = ldstr2(lds + bo[2 * pass + 1][off >> 16] + (off & 65535), 2048);
    };
    bread(IC<0>{}); bread(IC<1>{}); bread(IC<2>{});
    SBAR();
    publish<4>(c, EV_PUB3, flag_off(2, rb3 * 8 + cb3));
    STAMP(2, 10);
    { const int l = lane < 14 ? lane : 0; acquire(c, flag_off(2, (rb4 * 2 + (l >= 7)) * 8 + (l >= 7 ? l - 7 : l)), EV_GO4); }
    STAMP(3, 0);
    const int rot = p4_rot(cb4);
    const unsigned sa0 = OFF_H + (unsigned)(t4 * 28 + rot * 4) * 1024u;
    auto sa = [&](int g) { return sa0 + g * 4096u - (g + rot >= 7 ? 28672u : 0u); };
    const unsigned sg = OFF_G + (unsigned)(t4 * 112 + cb4 * 8) * 1024u;
    f32x2 sv[7];
    static_for<0, 12>([&](auto kc) { constexpr int k = decltype(kc)::value; ld16<(k & 3) * 1024>(a[k], rs, va, sa(k >> 2)); });
    f32x16 acc[4] = {};
    u32x4 og, rf;
    static_for<0, 56>([&](auto kc) {
      constexpr int k = decltype(kc)::value, pass = k / 28, ks = k % 28;
      if constexpr (k < 28) waitv<((k + 11 < 27 ? k + 11 : 27) - k + (k >= 9 && k <= 20 ? 7 : 0))>(a[k]);
      if constexpr (k == 28) {
#pragma unroll
        for (int i = 0; i < 4; ++i) acc[i] = MFMA(frag((u32x4){cf[i][0], cf[i][1], 0u, 0u}), frag(rf), acc[i]);
        SBAR();
      }
      acc[2 * pass] = MFMA(frag(bq[k & 3][0]), frag(a[ks]), acc[2 * pass]);
      SBAR();
      if constexpr (k + 12 < 28) ld16<((k + 12) & 3) * 1024>(a[k + 12], rs, va, sa((k + 12) >> 2));
      if constexpr (k == 8) stats_issue(c, t4, sv);
      if constexpr (k + 3 < 56) bread(IC<k + 3>{});
      SBAR();
      acc[2 * pass + 1] = MFMA(frag(bq[k & 3][1]), frag(a[ks]), acc[2 * pass + 1]);
      SBAR();
      if constexpr (k == 21) {
        stats_pin(sv);
        float t = sv[0][0];
#pragma unroll
        for (int i = 1; i < 7; ++i) { t += sv[i][0]; pinf(t); }
        rf = rowfrag(xhalf_sum(t) * (-1.f / 448.f));
        pin4(rf);
        SBAR();
      }
      if constexpr (k == 24) { ev_set(c, EV_A4, w); SBAR(); }
      if constexpr (k >= 30 && k < 46) {
        constexpr int e = k - 30;
        og[e & 3] = relu2(acc[e >> 3][(e & 7) * 2], acc[e >> 3][(e & 7) * 2 + 1]);
        PIN(og);
        if constexpr ((e & 3) == 3) st16<(e >> 2) * 1024>(og, rs, va, sg);
        SBAR();
      }
    });
    ev_set(c, EV_X4, w);
    STAMP(3, 8);
    static_for<0, 4>([&](auto fc) {
      constexpr int f = decltype(fc)::value;
      u32x4 o;
#pragma unroll
      for (int e = 0; e < 4; ++e) o[e] = relu2(acc[2 + (f >> 1)][8 * (f & 1) + 2 * e], acc[2 + (f >> 1)][8 * (f & 1) + 2 * e + 1]);
      st16<f * 1024>(o, rs, va, sg + 4096u);
    });
    STAMP(3, 9);
    publish<4>(c, EV_PUB4, flag_off(3, rb4 * 16 + cb4));
    STAMP(3, 10);
  }
  {
    unsigned bo[2][3];
#pragma unroll
    for (int i = 0; i < 2; ++i) {
      bo[i][0] = kh * 2048 + (4 * h + q) * 128 + ((kh ^ i ^ (q >> 1)) * 64) + trl; bo[i][1] = bo[i][0] + 65536; bo[i][2] = bo[i][0] + 131072;
      PIN(bo[i][0]); PIN(bo[i][1]); PIN(bo[i][2]);
    }
    acquire(c, flag_off(3, (rb3 >> 1) * 16 + (lane < 14 ? lane : 0)), EV_GO5);
    STAMP(4, 0);
    const int rot = p5_rot(cb3);
    const unsigned sa0 = OFF_G + (unsigned)(t3 * 112 + kh * 56 + rot * 4) * 1024u;
    auto sa = [&](int g) { return sa0 + g * 4096u - (g + rot >= 14 ? 57344u : 0u); };
    u32x4 a[W5], bq[4][2];
    f32x2 sv[7];
    static_for<0, W5>([&](auto kc) { constexpr int k = decltype(kc)::value; ld16<(k & 3) * 1024>(a[k], rs, va, sa(k >> 2)); });
    auto bread = [&](auto kc) {
      constexpr int k = decltype(kc)::value, off = w2_pos(k) * 4096;
      bq[k & 3][0] = ldstr2(lds + bo[0][off >> 16] + (off & 65535), 1024);
      bq[k & 3][1] = ldstr2(lds + bo[1][off >> 16] + (off & 65535), 1024);
    };
    ev_wait_half(c, EV_W2A);
    bread(IC<0>{}); bread(IC<1>{}); bread(IC<2>{});
    f32x16 acc[2] = {};
    static_for<0, 56>([&](auto kc) {
      constexpr int k = decltype(kc)::value;
      waitv<(k <= 56 - W5 ? W5 - 1 : 55 - k + 7)>(a[k % W5]);
      acc[1] = MFMA(frag(bq[k & 3][1]), frag(a[k % W5]), acc[1]);
      SBAR();
      if constexpr (k == 4 || k == 11 || k == 18) ev_set(c, EV_SEG0 + (k - 4) / 7, w);
      if constexpr (k == 32) { pin16(acc[0]); pin16(acc[1]); ev_wait_half(c, EV_W2B); }
      if constexpr (k + 3 < 56) bread(IC<k + 3>{});
      SBAR();
      acc[0] = MFMA(frag(bq[k & 3][0]), frag(a[k % W5]), acc[0]);
      SBAR();
      if constexpr (k + W5 < 56) ld16<((k + W5) & 3) * 1024>(a[k % W5], rs, va, sa((k + W5) >> 2));
      if constexpr (k == 56 - W5) stats_issue(c, t3, sv);
      SBAR();
    });
    STAMP(4, 15);
    float mu, rstd;
    STATS_WAIT(0, sv);
    stats_finish(sv, mu, rstd);
    pair_reduce(c, acc, L_XB, EV_RED5);
    STAMP(4, 16);
#pragma unroll
    for (int sx = 0; sx < 2; ++sx) {
      u32x4 o;
#pragma unroll
      for (int i = 0; i < 4; ++i) o[i] = pk2(acc[0][8 * sx + 2 * i] * rstd + bflo(hres[sx][i]), acc[0][8 * sx + 2 * i + 1] * rstd + bfhi(hres[sx][i]));
      auto x0 = __builtin_amdgcn_permlane32_swap(o[0], o[2], false, false);
      auto x1 = __builtin_amdgcn_permlane32_swap(o[1], o[3], false, false);
      *(u32x4*)(out + (unsigned)(t3 * 32 + r) * 896u + (sub * 32 + 16 * sx + 8 * h) * 2) = (u32x4){x0[0], x1[0], x0[1], x1[1]};
    }
    STAMP(4, 18);
  }
}

__device__ __forceinline__ void helper(const Ctx& c, const char* wo, const char* w1, const char* w2, int cb3, int cb4) {
  const int lane = c.lane, w = c.w;
  const rsrc_t rwo = mkrs(wo, D_ * D_ * 2), rw1 = mkrs(w1, D_ * DFF * 2), rw2 = mkrs(w2, D_ * DFF * 2);
  const unsigned vw1 = (unsigned)(lane >> 4) * 3584u + (unsigned)((lane & 15) ^ ((lane >> 4) << 2)) * 16u;
  const unsigned vw2 = (unsigned)(lane >> 3) * 896u + (unsigned)((lane & 7) ^ (((lane >> 4) & 1) << 2)) * 16u;
  const int rot5 = p5_rot(cb3) * 4;
  const unsigned sw0 = (unsigned)((w >> 1) * 896 + (w & 1) * 8 + rot5 * 16) * 896u + cb3 * 128u;
  auto sw = [&](int k) { return sw0 + k * 14336u - (k + rot5 >= 56 ? 56u * 14336u : 0u); };
  u32x4 s1[16], st[49];
  auto ldw = [&](auto ic) { constexpr int i = decltype(ic)::value, k = i < 28 ? i : i + 7; ld16p(st[i], rw2, vw2, sw(k)); };
  ev_wait<1, 4>(c, EV_GO2);
  __builtin_amdgcn_s_sleep(32);
#pragma unroll 1
  for (int p = w; p < 57; p += 4) { dma16(rwo, vw2, p * 7168u + cb3 * 128u, c.lbase + L_WO + p * 1024); __builtin_amdgcn_s_sleep(PACE); }
#pragma unroll 1
  for (int p = w; p < 48; p += 4) { dma16(rw1, vw1, w1_piece(p, cb4), c.lbase + p * 1024); __builtin_amdgcn_s_sleep(PACE); }
  static_for<0, 16>([&](auto ic) { constexpr int i = decltype(ic)::value; ld16p(s1[i], rw1, vw1, w1_piece(48 + i * 4 + w, cb4)); __builtin_amdgcn_s_sleep(PACE); });
  static_for<0, 42>([&](auto ic) {
    ldw(ic);
    __builtin_amdgcn_s_sleep(PACE);
    if constexpr (decltype(ic)::value == 15) { waitv<16>(s1[15]); ev_set(c, EV_WO, w); }
  });
  STAMP(1, 16);
  unsigned wb[3] = {(unsigned)(w * 1024 + lane * 16), 0u, 0u};
  wb[1] = wb[0] + 65536; wb[2] = wb[0] + 131072;
  PIN(wb[0]); PIN(wb[1]); PIN(wb[2]);
  ev_wait<4, 1>(c, EV_BPRE);
  static_for<0, 16>([&](auto ic) {
    constexpr int i = decltype(ic)::value, off = 3 * SLOT + i * 4096;
    waitv<42>(s1[i]);
    *(LAS u32x4*)(c.lds + wb[off >> 16] + (off & 65535)) = s1[i];
  });
  ev_set(c, EV_W1, w);
  ev_wait<4>(c, EV_W1);
  {
    const int q = (lane & 15) >> 2;
    lptr bp = c.lds + (4 * c.h + q) * 256 + ((w ^ q) * 64) + ((lane >> 4) & 1) * 32 + (lane & 3) * 8;
    float cs[4] = {0.f, 0.f, 0.f, 0.f};
#pragma unroll 1
    for (int sl = 0; sl < 7; ++sl) {
      u32x2 b[8];
#pragma unroll
      for (int k = 0; k < 8; ++k) b[k] = ldstr(bp + sl * SLOT + k * 2048);
      SBAR();
#pragma unroll
      for (int k = 0; k < 8; ++k) { cs[(k & 1) * 2] = dot2(b[k][0], ONES2, cs[(k & 1) * 2]); cs[(k & 1) * 2 + 1] = dot2(b[k][1], ONES2, cs[(k & 1) * 2 + 1]); }
    }
    *(LAS float*)(c.lds + L_CS + w * 128 + c.r * 4) = xhalf_sum((cs[0] + cs[1]) + (cs[2] + cs[3]));
    ev_set(c, EV_CS, w);
  }
  ev_wait<4, 2>(c, EV_A4);
#pragma unroll 1
  for (int k = 28; k < 35; ++k) dma16(rw2, vw2, sw(k), c.lbase + k * 4096 + w * 1024);
  static_for<42, 49>(ldw);
  auto stw = [&](auto ic) {
    constexpr int i = decltype(ic)::value, k = i < 28 ? i : i + 7, off = w2_pos(k) * 4096;
    waitv<0>(st[i]);
    *(LAS u32x4*)(c.lds + wb[off >> 16] + (off & 65535)) = st[i];
  };
  ev_wait<4, 2>(c, EV_X4);
  static_for<0, 28>(stw);
  ev_set(c, EV_W2A, w);
  ev_wait_half<2>(c, EV_SEG0);
  static_for<28, 35>(stw);
  ev_wait_half<2>(c, EV_SEG1);
  static_for<35, 42>(stw);
  ev_wait_half<2>(c, EV_SEG2);
  static_for<42, 49>(stw);
  ev_set(c, EV_W2B, w);
}

__global__ __launch_bounds__(512, 2) void block_fwd(const char* __restrict__ x, const char* __restrict__ wqkv, const char* __restrict__ wo,
                                                    const char* __restrict__ w1, const char* __restrict__ w2,
                                                    char* __restrict__ ws, char* __restrict__ out) {
  extern __shared__ __attribute__((aligned(16))) char smem[];
  unsigned long long pc;
  asm volatile("s_getpc_b64 %0" : "=s"(pc));
  asm volatile("" :: "s"(x), "s"(wqkv), "s"(wo), "s"(w1), "s"(w2), "s"(ws), "s"(out));
  Ctx c;
  c.lds = (lptr)smem; c.lbase = (unsigned)(uintptr_t)smem; c.ws = ws;
  c.rws = mkrs(ws, WS_USED);
  c.lane = threadIdx.x & 63; c.w8 = __builtin_amdgcn_readfirstlane(threadIdx.x >> 6); c.w = c.w8 & 3;
  c.r = c.lane & 31; c.h = c.lane >> 5;
  const int xcd = blockIdx.x & 7, j = blockIdx.x >> 3;
  const int rb1 = (xcd >> 1) * 4 + (j & 3), cb1 = (xcd & 1) * 8 + (j >> 2);
  const int rb3 = xcd * 4 + (j & 3), cb3 = j >> 2, rb4 = xcd * 2 + (j & 1), cb4 = j >> 1;
  c.tag = 0;
  if (c.w8 >= 4) {
    STAMP(0, 0);
    p1_load(c, x, wqkv, wo, w1, w2, rb1, cb1);
    if (j < 28) helper(c, wo, w1, w2, cb3, cb4);
    return;
  }
  if (threadIdx.x < EV_COUNT * 4) *(LAS unsigned*)(c.lds + L_CNT + threadIdx.x * 4) = 0;
  {
    const void* tp = j < 5 ? (const void*)ws : (j == 5 ? (const void*)out : (j == 6 ? (const void*)wo : (j == 7 ? (const void*)w1 : (j == 8 ? (const void*)w2 : (const void*)(pc & ~255ull)))));
    const unsigned tb = c.w8 != 3 ? 0u : (j < 5 ? WS_USED : (j == 5 ? S_ * D_ * 2 : (j == 6 ? D_ * D_ * 2 : (j < 9 ? D_ * DFF * 2 : CODE_BYTES))));
    c.rwarm = mkrs(tp, tb);
    c.vwarm = c.lane * (j < 9 ? 65536u : 128u);
    c.swarm = j < 5 ? j * (64u * 65536u) : (j < 9 ? 0u : (j - 9) * 8192u);
  }
  if (cb1 < 10) p1_compute<3>(c, x, rb1, cb1);
  else if (cb1 == 10) p1_compute<2>(c, x, rb1, cb1);
  else p1_compute<0>(c, x, rb1, cb1);
  publish<4>(c, EV_PUB1, flag_off(0, cb1 * 16 + rb1));
  STAMP(0, 10);
  u32x2 xr[4];
  {
    const rsrc_t rx = mkrs(x, S_ * D_ * 2);
    const unsigned vx = (unsigned)c.r * 896u + c.h * 8, sx = (unsigned)(rb3 * 2 + (c.w & 1)) * (32u * 896u) + (cb3 * 2 + (c.w >> 1)) * 64u;
#pragma unroll
    for (int g = 0; g < 4; ++g) xr[g] = __builtin_amdgcn_raw_buffer_load_b64(rx, vx + sx + g * 16, 0, 0);
  }
  p2_attn(c);
  if (j >= 28) {
    p2_publish(c);
    if (blockIdx.x == 255 && c.w8 == 0) {
#pragma unroll 1
      for (int i = 0; i < 4; ++i) poll(c, flag_off(0, i * 64 + c.lane));
      if (c.lane == 0) __builtin_amdgcn_raw_buffer_store_b32(c.tag, c.rws, 0, OFF_EP, 16);
    }
    return;
  }
  p345_compute(c, xr, out, rb3, cb3, rb4, cb4);
}

extern "C" void kernel_launch(void* const* d_in, const int* in_sizes, int n_in, void* d_out, int out_size,
                              void* d_ws, size_t ws_size, hipStream_t stream) {
  (void)hipFuncSetAttribute((const void*)block_fwd, hipFuncAttributeMaxDynamicSharedMemorySize, LDS_BYTES);
  block_fwd<<<256, 512, LDS_BYTES, stream>>>((const char*)d_in[0], (const char*)d_in[1], (const char*)d_in[2],
                                             (const char*)d_in[4], (const char*)d_in[5], (char*)d_ws, (char*)d_out);
}
```

```cpp
#include <hip/hip_runtime.h>
#include <hip/hip_bf16.h>
#include <stdint.h>

typedef __attribute__((ext_vector_type(8))) short bf16x8;
typedef __attribute__((ext_vector_type(4))) short s16x4;
typedef __attribute__((ext_vector_type(2))) short s16x2;
typedef __attribute__((ext_vector_type(2))) __bf16 bf16x2;
typedef __attribute__((ext_vector_type(16))) float f32x16;
typedef __attribute__((ext_vector_type(8))) float f32x8;
typedef __attribute__((ext_vector_type(4))) float f32x4;
typedef __attribute__((ext_vector_type(2))) float f32x2;
typedef __attribute__((ext_vector_type(4))) unsigned u32x4;
typedef __attribute__((ext_vector_type(2))) unsigned u32x2;
typedef __amdgpu_buffer_rsrc_t rsrc_t;
#define LAS __attribute__((address_space(3)))
typedef LAS char* lptr;

constexpr int S_ = 2048, D_ = 448, DFF = 1792;
constexpr unsigned QKV_BYTES = 16u * 64u * 2048u;
constexpr unsigned OFF_Q = 0, OFF_K = QKV_BYTES, OFF_V = 2 * QKV_BYTES;
constexpr unsigned OFF_F1 = 3 * QKV_BYTES;
constexpr unsigned OFF_EP = OFF_F1 + 65536u;
constexpr unsigned OFF_PR = OFF_EP + 256u;
constexpr unsigned OFF_LN = OFF_PR + 256u;
constexpr unsigned OFF_A = 8u << 20, OFF_B = 24u << 20, WS_USED = 32u << 20;
constexpr unsigned POS_G = 0, POS_ST = 8u * 112u * 1024u, POS_FL = POS_ST + 8u * 4096u;
constexpr unsigned POS_O = 0, POS_H = 8u * 32768u;
constexpr unsigned OOB = 0x7F000000u;
constexpr int SLOT = 16384, L_XA = 3 * SLOT, L_WO = 6 * SLOT, L_ML = L_WO + 57 * 1024, L_XB = 35 * 4096, LDS_BYTES = 163840;
constexpr int L_CS = L_ML + 4096, L_ROW = L_CS + 1536, L_CNT = LDS_BYTES - 1024, L_DUMMY = L_CNT + 512;
constexpr int P1_SLOT = 16384 + 12288;
enum { EV_PUB1, EV_PUB2, EV_PUB3, EV_PUB4, EV_GO2, EV_GO3, EV_GO4, EV_GO5, EV_XA, EV_WO, EV_BPRE, EV_W1, EV_CS,
       EV_P1D, EV_TAG, EV_MAP, EV_RED3, EV_A4, EV_X4, EV_W2A, EV_SEG0, EV_SEG1, EV_SEG2, EV_W2B, EV_RED5, EV_COUNT };

constexpr unsigned CODE_BYTES = 51200;
constexpr int PACE = 2;
#define W5 14
constexpr float QSCALE = 0.18898223650461362f * 1.4426950408889634f;
constexpr float EPS = 1e-5f;
constexpr unsigned ONES2 = 0x3F803F80u;

#define MFMA(a, b, c) __builtin_amdgcn_mfma_f32_32x32x16_bf16((a), (b), (c), 0, 0, 0)
#define WAITV_BAR(N) asm volatile("s_waitcnt vmcnt(" #N ")\n\ts_barrier" ::: "memory")
#define BAR() asm volatile("s_waitcnt lgkmcnt(0)\n\ts_barrier" ::: "memory")
#define SBAR() __builtin_amdgcn_sched_barrier(0)
#define PIN(x) asm volatile("" : "+v"(x))
#define EX(v) __builtin_amdgcn_exp2f(v)
#define STAMP(kid, i) do {} while (0)

__device__ __forceinline__ rsrc_t mkrs(const void* p, unsigned bytes) {
  return __builtin_amdgcn_make_buffer_rsrc((void*)p, (short)0, (int)bytes, 0x00020000);
}
__device__ __forceinline__ void dma16(rsrc_t r, unsigned voff, unsigned soff, unsigned ldsaddr) {
  asm volatile("s_mov_b32 m0, %0\n\ts_nop 0\n\tbuffer_load_dwordx4 %1, %2, %3 offen lds"
               :: "s"(ldsaddr), "v"(voff), "s"(r), "s"(soff) : "memory");
}
__device__ __forceinline__ void dma16x4_sc1(rsrc_t r, unsigned voff, unsigned soff, unsigned ldsaddr) {
  asm volatile("s_mov_b32 m0, %0\n\ts_nop 0\n\tbuffer_load_dwordx4 %1, %2, %3 offen sc1 lds\n\t"
               "buffer_load_dwordx4 %1, %2, %3 offen offset:1024 sc1 lds\n\t"
               "buffer_load_dwordx4 %1, %2, %3 offen offset:2048 sc1 lds\n\t"
               "buffer_load_dwordx4 %1, %2, %3 offen offset:3072 sc1 lds"
               :: "s"(ldsaddr), "v"(voff), "s"(r), "s"(soff) : "memory");
}
template <int IMM>
__device__ __forceinline__ void ld16(u32x4& d, rsrc_t r, unsigned voff, unsigned soff) {
  asm volatile("buffer_load_dwordx4 %0, %1, %2, %3 offen offset:%4 sc1" : "=v"(d) : "v"(voff), "s"(r), "s"(soff), "i"(IMM) : "memory");
}
__device__ __forceinline__ void ld16p(u32x4& d, rsrc_t r, unsigned voff, unsigned soff) {
  asm volatile("buffer_load_dwordx4 %0, %1, %2, %3 offen" : "=v"(d) : "v"(voff), "s"(r), "s"(soff) : "memory");
}
template <int N>
__device__ __forceinline__ void waitv(u32x4& x) { asm volatile("s_waitcnt vmcnt(%1)" : "+v"(x) : "i"(N) : "memory"); }
template <int IMM>
__device__ __forceinline__ void st16(u32x4 v, rsrc_t r, unsigned voff, unsigned soff) {
  asm volatile("buffer_store_dwordx4 %0, %1, %2, %3 offen offset:%4 sc1\n\ts_nop 1" :: "v"(v), "v"(voff), "s"(r), "s"(soff), "i"(IMM) : "memory");
}
__device__ __forceinline__ void st16s(int s, u32x4 v, rsrc_t r, unsigned voff, unsigned soff) {
  if (s) st16<1024>(v, r, voff, soff); else st16<0>(v, r, voff, soff);
}
__device__ __forceinline__ unsigned pk2(float a, float b) {
  f32x2 v = {a, b};
  return __builtin_bit_cast(unsigned, __builtin_convertvector(v, bf16x2));
}
__device__ __forceinline__ float bflo(unsigned u) { return __uint_as_float(u << 16); }
__device__ __forceinline__ float bfhi(unsigned u) { return __uint_as_float(u & 0xffff0000u); }
__device__ __forceinline__ float dot2(unsigned a, unsigned b, float c) {
  return __builtin_amdgcn_fdot2_f32_bf16(__builtin_bit_cast(bf16x2, a), __builtin_bit_cast(bf16x2, b), c, false);
}
__device__ __forceinline__ u32x4 lds128(lptr p) { return *(LAS u32x4*)p; }
__device__ __forceinline__ f32x4 ldsf4(lptr p) { return *(LAS f32x4*)p; }
__device__ __forceinline__ u32x2 ldstr(lptr p) {
  return __builtin_bit_cast(u32x2, __builtin_amdgcn_ds_read_tr16_b64_v4i16((LAS s16x4*)p));
}
__device__ __forceinline__ u32x4 ldstr2(lptr p, int d) {
  const u32x2 a = ldstr(p), b = ldstr(p + d);
  return (u32x4){a[0], a[1], b[0], b[1]};
}
__device__ __forceinline__ bf16x8 frag(u32x4 v) { return __builtin_bit_cast(bf16x8, v); }
__device__ __forceinline__ float xhalf_sum(float v) {
  auto r = __builtin_amdgcn_permlane32_swap(__float_as_uint(v), __float_as_uint(v), false, false);
  return __uint_as_float(r[0]) + __uint_as_float(r[1]);
}
__device__ __forceinline__ float xhalf_max(float v) {
  auto r = __builtin_amdgcn_permlane32_swap(__float_as_uint(v), __float_as_uint(v), false, false);
  return fmaxf(__uint_as_float(r[0]), __uint_as_float(r[1]));
}
__device__ __forceinline__ u32x4 colfrag(float cs, int h) {
  const unsigned a = pk2(cs, cs);
  const float lo = cs - bflo(a);
  const unsigned b = pk2(lo, lo);
  return (u32x4){h ? 0u : a, h ? 0u : b, 0u, 0u};
}
__device__ __forceinline__ u32x4 rowfrag(float nmu) {
  const unsigned a = pk2(nmu, nmu);
  const unsigned p = pk2(nmu, nmu - bflo(a));
  return (u32x4){p, p, 0u, 0u};
}

__device__ __forceinline__ void pin16(f32x16& x) { PIN(x); }
__device__ __forceinline__ void pin4(u32x4& x) { PIN(x); }
__device__ __forceinline__ void pinf(float& x) { PIN(x); }
template <int V> struct IC { static constexpr int value = V; };
template <int I, int N, class F>
__device__ __forceinline__ void static_for(F&& f) {
  if constexpr (I < N) { f(IC<I>{}); static_for<I + 1, N>(f); }
}

struct Ctx {
  lptr lds; unsigned lbase;
  char* ws;
  rsrc_t rws;
  unsigned tag, map;
  char* wb;
  rsrc_t rwarm; unsigned vwarm, swarm;
  int lane, w8, w, r, h;
};

__device__ __forceinline__ constexpr unsigned spread(unsigned L) { return L + (L & ~8191u); }
__device__ __forceinline__ unsigned base_a(const Ctx& c, unsigned g) { return OFF_A + (g << 21) + (((c.map >> g) & 1u) << 13); }
__device__ __forceinline__ unsigned base_b(const Ctx& c, unsigned g) { return OFF_B + (g << 20) + (((c.map >> (8 + g)) & 1u) << 13); }
__device__ __forceinline__ void stampf(const Ctx& c, int kid, int i) { STAMP(kid, i); }
__device__ __forceinline__ void ev_set(const Ctx& c, int idx, int word, unsigned one = 1) {
  const unsigned addr = c.lbase + L_CNT + idx * 16 + word * 4;
  asm volatile("s_mov_b64 exec, 1\n\tds_write_b32 %0, %1\n\ts_mov_b64 exec, -1" :: "v"(addr), "v"(one) : "memory");
}
template <int NWORDS, int SLEEP = 0>
__device__ __forceinline__ void ev_wait(const Ctx& c, int idx) {
  const unsigned addr = c.lbase + L_CNT + idx * 16;
#pragma unroll 1
  for (int spins = 0; spins < (1 << 24); ++spins) {
    u32x4 v;
    asm volatile("ds_read_b128 %0, %1\n\ts_waitcnt lgkmcnt(0)" : "=v"(v) : "v"(addr) : "memory");
    if (__builtin_amdgcn_readfirstlane(NWORDS == 4 ? (v[0] & v[1] & v[2] & v[3]) : v[0])) break;
    if (SLEEP) __builtin_amdgcn_s_sleep(SLEEP);
  }
}
template <int SLEEP = 0>
__device__ __forceinline__ void ev_wait_half(const Ctx& c, int idx) {
  const unsigned addr = c.lbase + L_CNT + idx * 16 + (c.w >> 1) * 8;
#pragma unroll 1
  for (int spins = 0; spins < (1 << 24); ++spins) {
    u32x2 v;
    asm volatile("ds_read_b64 %0, %1\n\ts_waitcnt lgkmcnt(0)" : "=v"(v) : "v"(addr) : "memory");
    if (__builtin_amdgcn_readfirstlane(v[0] & v[1])) break;
    if (SLEEP) __builtin_amdgcn_s_sleep(SLEEP);
  }
}
template <int NW>
__device__ __forceinline__ void publish(const Ctx& c, int idx, unsigned flag_off) {
  const unsigned addr = c.lbase + L_CNT + idx * 16, one = 1;
  unsigned old;
  asm volatile("s_waitcnt vmcnt(0)\n\ts_mov_b64 exec, 1\n\tds_add_rtn_u32 %0, %1, %2\n\ts_mov_b64 exec, -1\n\ts_waitcnt lgkmcnt(0)"
               : "=v"(old) : "v"(addr), "v"(one) : "memory");
  if (__builtin_amdgcn_readfirstlane(old) == NW - 1) {
    const unsigned tag = c.tag;
    asm volatile("s_mov_b64 exec, 1\n\tbuffer_store_dword %0, off, %1, %2 sc1\n\ts_mov_b64 exec, -1" :: "v"(tag), "s"(c.rws), "s"(flag_off) : "memory");
  }
}
__device__ __forceinline__ void poll(const Ctx& c, unsigned voff) {
#pragma unroll 1
  for (int spins = 0; spins < (1 << 17); ++spins) {
    unsigned f;
    asm volatile("buffer_load_dword %0, %1, %2, 0 offen sc1\n\ts_waitcnt vmcnt(0)" : "=v"(f) : "v"(voff), "s"(c.rws) : "memory");
    if (__all(f == c.tag)) break;
  }
}
__device__ __forceinline__ void acquire(const Ctx& c, unsigned voff, int ev) {
  if (c.w == 0) { poll(c, voff); ev_set(c, ev, 0); }
  else ev_wait<1>(c, ev);
}
__device__ __forceinline__ unsigned flag1_off(int idx) { return OFF_F1 + (unsigned)idx * 256u; }
__device__ __forceinline__ unsigned flag_off(const Ctx& c, int phase, int idx, unsigned grp) { return base_a(c, grp) + spread(POS_FL + (phase - 1) * 8192u) + (idx & 31) * 256u; }
__device__ __forceinline__ void touch_lds(const Ctx& c, rsrc_t r, unsigned voff, unsigned soff) {
  asm volatile("s_mov_b32 m0, %0\n\ts_nop 0\n\tbuffer_load_dword %1, %2, %3 offen lds" :: "s"(c.lbase + L_DUMMY), "v"(voff), "s"(r), "s"(soff) : "memory");
}

template <int CTRL>
__device__ __forceinline__ float dpp_add(float v) {
  return v + __int_as_float(__builtin_amdgcn_update_dpp(0, __float_as_int(v), CTRL, 0xf, 0xf, true));
}
__device__ __forceinline__ float half_sum(float v) {
  v = dpp_add<0x140>(dpp_add<0x141>(dpp_add<0x4E>(dpp_add<0xB1>(v))));
  auto r = __builtin_amdgcn_permlane16_swap(__float_as_uint(v), __float_as_uint(v), false, false);
  return __uint_as_float(r[0]) + __uint_as_float(r[1]);
}

template <int NQ>
__device__ __forceinline__ void p1_compute(Ctx& c, const char* x, int rb, int cb) {
  const int lane = c.lane, w = c.w, r = c.r, h = c.h;
  lptr lds = c.lds;
  const int srow_g = rb * 128 + cb * 8 + w * 2 + h;
  u32x4 xs0, xs1;
  {
    const rsrc_t rx = mkrs(x, S_ * D_ * 2);
    const unsigned vo = r < 28 ? (unsigned)srow_g * 896u + r * 32u : OOB;
    ld16p(xs0, rx, vo, 0); ld16p(xs1, rx, vo, 16);
    touch_lds(c, c.rwarm, c.vwarm, c.swarm);
  }
  const int arow = w * 32 + r;
  const unsigned aoff = arow * 128 + ((h ^ ((arow >> 1) & 7)) * 16);
  const unsigned boff = 16384 + (8 * h + ((lane & 15) >> 2)) * 192 + ((lane >> 4) & 1) * 32 + (lane & 3) * 8 + (cb & 1) * 8;
  unsigned ao[4], bo[4];
#pragma unroll
  for (int i = 0; i < 4; ++i) { const int ks = (w + i) & 3; ao[i] = aoff ^ (ks * 32); bo[i] = boff + ks * 3072; }
  f32x16 acc[3] = {};
  float cs[3] = {0.f, 0.f, 0.f};
  u32x4 a[4], b[4][3];
  auto dots = [&]() {
#pragma unroll
    for (int e = 0; e < 4; ++e)
#pragma unroll
      for (int n = 0; n < 3; ++n) cs[n] = dot2(b[0][n][e], ONES2, cs[n]);
    PIN(cs[0]); PIN(cs[1]); PIN(cs[2]);
    SBAR();
  };
  {
    unsigned prev;
    asm volatile("s_load_dword %0, %1, 0x0\n\ts_waitcnt lgkmcnt(0)" : "=s"(prev) : "s"(c.wb + OFF_EP) : "memory");
    c.tag = prev * 0x9E3779B1u + 0x7F4A7C15u;
  }
  {
    asm volatile("s_waitcnt vmcnt(1)" : "+v"(xs0), "+v"(xs1) :: "memory");
    float s0 = 0.f, s1 = 0.f, q0 = 0.f, q1 = 0.f;
#pragma unroll
    for (int e = 0; e < 4; ++e) { s0 = dot2(xs0[e], ONES2, s0); s1 = dot2(xs1[e], ONES2, s1); q0 = dot2(xs0[e], xs0[e], q0); q1 = dot2(xs1[e], xs1[e], q1); }
    const float mu = half_sum(s0 + s1) * (1.f / 448.f);
    const float rstd = __builtin_amdgcn_rsqf(fmaxf(half_sum(q0 + q1) * (1.f / 448.f) - mu * mu, 0.f) + EPS);
    st16<0>((u32x4){__float_as_uint(-mu), c.tag, __float_as_uint(rstd), c.tag}, c.rws, r == 0 ? (unsigned)srow_g * 16u : OOB, OFF_LN);
    SBAR();
  }
  BAR();
  STAMP(0, 1);
#pragma unroll
  for (int i = 0; i < 4; ++i) {
#pragma unroll
    for (int n = 0; n < 3; ++n) b[i][n] = ldstr2(lds + bo[i] + n * 56, 768);
    a[i] = lds128(lds + ao[i]);
  }
  SBAR();
  u32x4 ln = {};
  const unsigned vln = (unsigned)(rb * 128 + arow) * 16u;
#pragma unroll 1
  for (int st = 1; st < 7; ++st) {
    lptr sb = lds + (st >= 5 ? st - 5 : st) * P1_SLOT;
    dots();
    asm volatile("s_barrier" ::: "memory");
#pragma unroll
    for (int i = 0; i < 4; ++i)
#pragma unroll
      for (int n = 0; n < 3; ++n) {
        acc[n] = n < NQ ? MFMA(frag(b[i][n]), frag(a[i]), acc[n]) : MFMA(frag(a[i]), frag(b[i][n]), acc[n]);
        SBAR();
        b[i][n] = ldstr2(sb + bo[i] + n * 56, 768);
        if (n == 2) a[i] = lds128(sb + ao[i]);
        if (i == 0 && n == 0) asm volatile("s_cmp_lg_u32 %4, 6\n\ts_cbranch_scc1 .Lnl%=\n\tbuffer_load_dwordx4 %0, %1, %2, %3 offen sc1\n.Lnl%=:" : "+v"(ln) : "v"(vln), "s"(c.rws), "s"(OFF_LN), "s"(st) : "memory", "scc");
        SBAR();
      }
  }
  STAMP(0, 7);
  dots();
#pragma unroll
  for (int i = 0; i < 2; ++i)
#pragma unroll
    for (int n = 0; n < 3; ++n) {
      acc[n] = n < NQ ? MFMA(frag(b[i][n]), frag(a[i]), acc[n]) : MFMA(frag(a[i]), frag(b[i][n]), acc[n]);
      if (i == 0) {
        const float t = xhalf_sum(cs[n]);
        *(LAS float*)(lds + L_CS + (n * 32 + r) * 16 + w * 4) = t;
      }
      SBAR();
    }
  BAR();
  f32x4 p4[3], r4[4];
  u32x4 cf[3];
#pragma unroll
  for (int n = 0; n < 3; ++n) p4[n] = ldsf4(lds + L_CS + (n * 32 + r) * 16);
  SBAR();
#pragma unroll
  for (int i = 2; i < 4; ++i)
#pragma unroll
    for (int n = 0; n < 3; ++n) {
      acc[n] = n < NQ ? MFMA(frag(b[i][n]), frag(a[i]), acc[n]) : MFMA(frag(a[i]), frag(b[i][n]), acc[n]);
      if (i == 3) {
        float t0 = p4[n][0] + p4[n][1], t1 = p4[n][2] + p4[n][3];
        PIN(t0); PIN(t1);
        cf[n] = colfrag(t0 + t1, h);
        PIN(cf[n]);
      }
      SBAR();
    }
  pin16(acc[0]); pin16(acc[1]); pin16(acc[2]);
  waitv<0>(ln);
#pragma unroll 1
  for (int spins = 0; spins < (1 << 17) && !__all(ln[1] == c.tag && ln[3] == c.tag); ++spins)
    asm volatile("buffer_load_dwordx4 %0, %1, %2, %3 offen sc1\n\ts_waitcnt vmcnt(0)" : "=v"(ln) : "v"(vln), "s"(c.rws), "s"(OFF_LN) : "memory");
  const float rstd = __uint_as_float(ln[2]);
  const u32x4 rf = rowfrag(__uint_as_float(ln[0]));
  if (NQ < 3) {
    lptr srow = lds + L_ROW + w * 128;
    *(LAS float*)(srow + r * 4) = rstd;
#pragma unroll
    for (int g = 0; g < 4; ++g) r4[g] = ldsf4(srow + (8 * g + 4 * h) * 4);
  }
#pragma unroll
  for (int n = 0; n < 3; ++n) acc[n] = n < NQ ? MFMA(frag(cf[n]), frag(rf), acc[n]) : MFMA(frag(rf), frag(cf[n]), acc[n]);
  STAMP(0, 8);
  const int t = rb * 4 + w;
#pragma unroll
  for (int n = 0; n < 3; ++n) {
    const int u = cb * 3 + n;
    const float sc = u < 16 ? rstd * QSCALE : rstd;
#pragma unroll
    for (int s = 0; s < 2; ++s) {
      u32x4 o;
#pragma unroll
      for (int i = 0; i < 4; ++i) {
        const int e = 8 * s + 2 * i;
        o[i] = n < NQ ? pk2(acc[n][e] * sc, acc[n][e + 1] * sc) : pk2(acc[n][e] * r4[e >> 2][e & 3], acc[n][e + 1] * r4[e >> 2][(e & 3) + 1]);
      }
      if (n < NQ) {
        if (s == 1 && h) { o[2] = u < 16 ? 0u : ONES2; o[3] = u < 16 ? 0u : 0x00003F80u; }
      } else if (r >= 28) {
        o = (u32x4){ONES2, ONES2, ONES2, ONES2};
      }
      st16s(s, o, c.rws, lane * 16, (unsigned)(u * 64 + t) * 2048u);
    }
  }
  STAMP(0, 9);
}

__device__ __forceinline__ void p1_load(const Ctx& c, const char* x, const char* wqkv, const char* wo, const char* w1, const char* w2, int rb, int cb) {
  const int lane = c.lane, w = c.w;
  const rsrc_t rx = mkrs(x, S_ * D_ * 2), rw = mkrs(wqkv, D_ * 3 * D_ * 2);
  unsigned vxa[2], vw[3];
#pragma unroll
  for (int p = 0; p < 2; ++p) vxa[p] = (unsigned)(lane >> 3) * 896u + (unsigned)((lane & 7) ^ ((4 * p + (lane >> 4)) & 7)) * 16u;
#pragma unroll
  for (int q = 0; q < 3; ++q) { const int i = q * 64 + lane; vw[q] = (unsigned)(i / 12) * 2688u + (unsigned)(i % 12) * 16u; }
  const unsigned sx0 = (unsigned)(rb * 128 + w * 32) * 896u, sw0 = (((unsigned)cb * 168u) & ~15u) + (unsigned)w * (16u * 2688u);
#define P1ROT 0
  int ks = P1ROT;
  auto issue = [&](int st) {
    const unsigned lb = c.lbase + (st >= 5 ? st - 5 : st) * P1_SLOT;
#pragma unroll
    for (int p = 0; p < 4; ++p) dma16(rx, vxa[p & 1], sx0 + ks * 128u + p * 7168u, lb + (w * 4 + p) * 1024);
#pragma unroll
    for (int q = 0; q < 3; ++q) dma16(rw, vw[q], sw0 + ks * (64u * 2688u), lb + 16384 + (w * 3 + q) * 1024);
    ks = ks == 6 ? 0 : ks + 1;
  };
  issue(0);
  touch_lds(c, rw, lane < 10 ? lane * 128 : OOB, (blockIdx.x * 4 + w) * 1176u);
  issue(1);
  WAITV_BAR(8);
#pragma unroll 1
  for (int st = 2; st < 7; ++st) {
    issue(st);
    if (st < 6) WAITV_BAR(7);
  }
  WAITV_BAR(7);
  WAITV_BAR(0);
  BAR();
  ev_wait<4, 1>(c, EV_P1D);
  const rsrc_t rt = mkrs(w == 0 ? wo : (w == 1 ? w1 : w2), w == 0 ? D_ * D_ * 2 : (w < 3 ? D_ * DFF * 2 : 0));
  touch_lds(c, rt, lane < (w ? 49 : 13) ? lane * 128u : OOB, blockIdx.x * (w ? 6272u : 1568u));
}

#define ATT_UNIT(SC, SN, KN0, KN1, QN, OPREV, VPREV1, OCUR, VCUR0, HOOK_A, HOOK_B, HOOK_C, HOOK_D)       \
  do {                                                                                                     \
    SN = MFMA(frag(KN0), frag(QN[0]), zero); SBAR();                                                       \
    p1[2] = pk2(e[4], e[5]); p1[3] = pk2(e[6], e[7]); PIN(p1); SBAR();                                     \
    e[0] = EX(SC[0]); e[1] = EX(SC[1]); e[2] = EX(SC[2]); e[3] = EX(SC[3]);                                \
    PIN(e); SBAR(); HOOK_A;                                                                                \
    OPREV = MFMA(frag(VPREV1), frag(p1), OPREV); SBAR();                                                   \
    p0[0] = pk2(e[0], e[1]); p0[1] = pk2(e[2], e[3]); PIN(p0); SBAR();                                     \
    e[4] = EX(SC[4]); e[5] = EX(SC[5]); e[6] = EX(SC[6]); e[7] = EX(SC[7]);                                \
    PIN(e); SBAR(); HOOK_B;                                                                                \
    SN = MFMA(frag(KN1), frag(QN[1]), SN); SBAR();                                                         \
    p0[2] = pk2(e[4], e[5]); p0[3] = pk2(e[6], e[7]); PIN(p0); SBAR();                                     \
    e[0] = EX(SC[8]); e[1] = EX(SC[9]); e[2] = EX(SC[10]); e[3] = EX(SC[11]);                              \
    PIN(e); SBAR(); HOOK_C;                                                                                \
    OCUR = MFMA(frag(VCUR0), frag(p0), OCUR); SBAR();                                                      \
    p1[0] = pk2(e[0], e[1]); p1[1] = pk2(e[2], e[3]); PIN(p1); SBAR();                                     \
    e[4] = EX(SC[12]); e[5] = EX(SC[13]); e[6] = EX(SC[14]); e[7] = EX(SC[15]);                            \
    PIN(e); SBAR(); HOOK_D;                                                                                \
  } while (0)

__device__ __forceinline__ int p4_rot(int cb4) { return cb4 >= 7 ? cb4 - 7 : cb4; }
#define P5ROT (cb3 & 1) * 6
__device__ __forceinline__ int p5_rot(int cb3) { return P5ROT; }
__device__ __forceinline__ unsigned w1_piece(int p, int cb4) {
  const int q = p + p4_rot(cb4) * 16;
  return (unsigned)(q >= 112 ? q - 112 : q) * 14336u + cb4 * 256u;
}
template <bool EXACT>
__device__ __forceinline__ void attn_pass(const Ctx& c, f32x16 (&o)[4], float (&m)[4]) {
  const int lane = c.lane, w = c.w, h = c.h;
  const int hd = (blockIdx.x & 7) * 2 + ((blockIdx.x >> 3) & 1), rb = blockIdx.x >> 4;
  const unsigned vo = lane * 16;
  const unsigned sq = OFF_Q + (unsigned)(hd * 64 + rb * 4) * 2048u;
  const unsigned sk = OFF_K + (unsigned)(hd * 64 + w * 16) * 2048u, sv = sk + QKV_BYTES;
  const unsigned rot = (unsigned)(rb & 7) * 4096u, rot1 = (rot + 4096u) & 32767u;
  const rsrc_t rs = c.rws;
  const f32x16 zero = {};
  u32x4 q0[2], q1[2], q2[2], q3[2], ka0, ka1, va0, va1, kb0, kb1, vb0, vb1, kc0, kc1, vc0, vc1, kd0, kd1, vd0, vd1;
  unsigned sqb[4];
#pragma unroll
  for (int b = 0; b < 4; ++b) { m[b] = -INFINITY; sqb[b] = sq + ((w + 1 + b) & 3) * 2048u; }
  u32x4 p0, p1;
  f32x8 e;
#pragma unroll 1
  for (int i = EXACT ? 15 : 0; i >= 0; --i) {
    const unsigned ko = sk + ((rot + i * 2048u) & 32767u);
    ld16<0>(ka0, rs, vo, ko); ld16<0>(q0[0], rs, vo, sqb[0]); ld16<1024>(ka1, rs, vo, ko); ld16<1024>(q0[1], rs, vo, sqb[0]);
    ld16<0>(q1[0], rs, vo, sqb[1]); ld16<1024>(q1[1], rs, vo, sqb[1]); ld16<0>(q2[0], rs, vo, sqb[2]); ld16<1024>(q2[1], rs, vo, sqb[2]);
    ld16<0>(q3[0], rs, vo, sqb[3]); ld16<1024>(q3[1], rs, vo, sqb[3]);
    ld16<0>(va0, rs, vo, sv + rot);
    ld16<1024>(va1, rs, vo, sv + rot); ld16<2048>(kb0, rs, vo, sk + rot); ld16<3072>(kb1, rs, vo, sk + rot); ld16<2048>(vb0, rs, vo, sv + rot);
#pragma unroll
    for (int b = 0; b < 4; ++b) { o[b] = zero; pin16(o[b]); }
    p0 = p1 = vd1 = (u32x4){}; e = f32x8{};
    PIN(p0); PIN(p1); PIN(vd1); PIN(e);
    asm volatile("s_waitcnt vmcnt(5)" : "+v"(q0[0]), "+v"(q0[1]), "+v"(q1[0]), "+v"(q1[1]), "+v"(q2[0]), "+v"(q2[1]), "+v"(q3[0]), "+v"(q3[1]),
                 "+v"(ka0), "+v"(ka1) :: "memory");
    f32x16 s0 = MFMA(frag(ka0), frag(q0[0]), zero), s1 = MFMA(frag(ka0), frag(q1[0]), zero);
    s0 = MFMA(frag(ka1), frag(q0[1]), s0); s1 = MFMA(frag(ka1), frag(q1[1]), s1);
    f32x16 s2 = MFMA(frag(ka0), frag(q2[0]), zero), s3 = MFMA(frag(ka0), frag(q3[0]), zero);
    s2 = MFMA(frag(ka1), frag(q2[1]), s2); s3 = MFMA(frag(ka1), frag(q3[1]), s3);
#pragma unroll
    for (int e_ = 0; e_ < 16; ++e_) { const int e = e_; m[0] = fmaxf(m[0], s0[e]); m[1] = fmaxf(m[1], s1[e]); m[2] = fmaxf(m[2], s2[e]); m[3] = fmaxf(m[3], s3[e]); }
  }
#pragma unroll
  for (int b = 0; b < 4; ++b) {
    m[b] = xhalf_max(m[b]);
    const float nm = -m[b];
    const unsigned u0 = __float_as_uint(nm) & 0xffff0000u;
    const float r1 = nm - __uint_as_float(u0);
    const unsigned u1 = __float_as_uint(r1) & 0xffff0000u;
    const float r2 = r1 - __uint_as_float(u1);
    u32x4& qq = b == 0 ? q0[1] : (b == 1 ? q1[1] : (b == 2 ? q2[1] : q3[1]));
    qq[2] = h ? ((u0 >> 16) | u1) : qq[2];
    qq[3] = h ? (__float_as_uint(r2) >> 16) : qq[3];
  }
  STAMP(1, 1);
  f32x16 sa = MFMA(frag(ka0), frag(q0[0]), zero), sb;
  sa = MFMA(frag(ka1), frag(q0[1]), sa);
  SBAR();
  ld16<3072>(vb1, rs, vo, sv + rot); ld16<0>(kc0, rs, vo, sk + rot1); ld16<1024>(kc1, rs, vo, sk + rot1); ld16<0>(vc0, rs, vo, sv + rot1);
  ld16<1024>(vc1, rs, vo, sv + rot1); ld16<2048>(kd0, rs, vo, sk + rot1); ld16<3072>(kd1, rs, vo, sk + rot1); ld16<2048>(vd0, rs, vo, sv + rot1);
  unsigned oc = rot;
#define ATT_TILE(K0, K1, V0, V1, KN0, KN1, VP1, VP1_OFF, VP1_S, OFF, KS, VS)                                              \
    ATT_UNIT(sa, sb, K0, K1, q1, o[3], VP1, o[0], V0, (void)0, ld16<VP1_OFF>(VP1, rs, vo, VP1_S),                         \
             asm volatile("s_waitcnt vmcnt(12)" : "+v"(V0), "+v"(V1) :: "memory"), (void)0);                              \
    ATT_UNIT(sb, sa, K0, K1, q2, o[0], V1, o[1], V0, (void)0, (void)0, (void)0, (void)0);                                 \
    ATT_UNIT(sa, sb, K0, K1, q3, o[1], V1, o[2], V0, ld16<OFF>(K0, rs, vo, KS), (void)0, ld16<OFF + 1024>(K1, rs, vo, KS), \
             asm volatile("s_waitcnt vmcnt(12)" : "+v"(KN0), "+v"(KN1) :: "memory"));                                     \
    ATT_UNIT(sb, sa, KN0, KN1, q0, o[2], V1, o[3], V0, (void)0, (void)0, (void)0, ld16<OFF>(V0, rs, vo, VS))
#pragma unroll 1
  for (int i = 0;; ++i) {
    const unsigned c1 = (oc + 4096u) & 32767u, n0 = (oc + 8192u) & 32767u, n1 = (oc + 12288u) & 32767u;
    const unsigned k0 = i < 3 ? sk + n0 : OOB, k1 = i < 3 ? sk + n1 : OOB, v0 = i < 3 ? sv + n0 : OOB, v1 = i < 3 ? sv + n1 : OOB;
    ATT_TILE(ka0, ka1, va0, va1, kb0, kb1, vd1, 3072, sv + c1, 0, k0, v0);
    ATT_TILE(kb0, kb1, vb0, vb1, kc0, kc1, va1, 1024, v0, 2048, k0, v0);
    ATT_TILE(kc0, kc1, vc0, vc1, kd0, kd1, vb1, 3072, v0, 0, k1, v1);
    if (i == 3) break;
    ATT_TILE(kd0, kd1, vd0, vd1, ka0, ka1, vc1, 1024, v1, 2048, k1, v1);
    oc = n0;
  }
  lptr xa[3];
  unsigned l16 = vo;
  PIN(l16);
#pragma unroll
  for (int b = 0; b < 3; ++b) xa[b] = c.lds + L_XA + (((w + 1 + b) & 3) * 3 + b) * 4096 + l16;
#define XA_OUT(B, G) do { *(LAS f32x4*)(xa[B] + (G) * 1024) = (f32x4){o[B][4 * (G)], o[B][4 * (G) + 1], o[B][4 * (G) + 2], o[B][4 * (G) + 3]}; SBAR(); } while (0)
  ATT_UNIT(sa, sb, kd0, kd1, q1, o[3], vc1, o[0], vd0, (void)0, (void)0,
           asm volatile("s_waitcnt vmcnt(0)" : "+v"(ka0), "+v"(ka1), "+v"(va0), "+v"(va1), "+v"(kb0), "+v"(kb1), "+v"(vb0), "+v"(vb1),
                        "+v"(kc0), "+v"(kc1), "+v"(vc0), "+v"(vd0), "+v"(vd1) :: "memory"), (void)0);
  ATT_UNIT(sb, sa, kd0, kd1, q2, o[0], vd1, o[1], vd0, (void)0, (void)0, (void)0, XA_OUT(0, 0));
  ATT_UNIT(sa, sb, kd0, kd1, q3, o[1], vd1, o[2], vd0, XA_OUT(0, 1), XA_OUT(0, 2), XA_OUT(0, 3), XA_OUT(1, 0));
  ATT_UNIT(sb, sa, kd0, kd1, q0, o[2], vd1, o[3], vd0, XA_OUT(1, 1), XA_OUT(1, 2), XA_OUT(1, 3), XA_OUT(2, 0));
  XA_OUT(2, 1);
  p1[2] = pk2(e[4], e[5]); p1[3] = pk2(e[6], e[7]);
  XA_OUT(2, 2);
  o[3] = MFMA(frag(vd1), frag(p1), o[3]);
  XA_OUT(2, 3);
}

__device__ __forceinline__ void p2_attn(Ctx& c) {
  const int lane = c.lane, w = c.w, r = c.r, h = c.h;
  lptr lds = c.lds;
  const int hd = (blockIdx.x & 7) * 2 + ((blockIdx.x >> 3) & 1), rb = blockIdx.x >> 4;
  {
    const int u = lane < 16 ? 16 + hd : (lane < 32 ? 32 + hd : hd);
    acquire(c, flag1_off((u / 3) * 16 + (lane < 32 ? (lane & 15) : rb)), EV_GO2);
  }
  STAMP(1, 0);
  f32x16 o[4];
  float m[4];
  __builtin_amdgcn_s_setprio(3);
  attn_pass<false>(c, o, m);
  bool ok = true;
#pragma unroll
  for (int b = 0; b < 4; ++b) ok &= o[b][12] >= 0x1p-30f && o[b][12] <= 0x1p30f;
  if (!__all(ok || h == 0)) attn_pass<true>(c, o, m);
  STAMP(1, 2);
#pragma unroll
  for (int b = 0; b < 4; ++b)
    *(LAS f32x2*)(lds + (h ? L_ML + ((w + 1 + b) & 3) * 1024 + r * 32 + b * 8 : L_CS + r * 8)) = (f32x2){m[b], o[b][12]};
  ev_set(c, EV_XA, w);
  pin16(o[3]);
  ev_wait<1>(c, EV_MAP);
  c.map = __builtin_amdgcn_readfirstlane(*(volatile LAS unsigned*)(lds + L_CNT + EV_MAP * 16));
  f32x4 a0, a1, v[3][4];
  {
    const unsigned fa = c.lbase + L_CNT + EV_XA * 16, ma = c.lbase + L_ML + w * 1024 + r * 32, xa = c.lbase + L_XA + w * 12288 + lane * 16;
#pragma unroll 1
    for (int spins = 0; spins < (1 << 24); ++spins) {
      u32x4 f;
      asm volatile("ds_read_b128 %0, %15\n\tds_read_b128 %1, %16\n\tds_read_b128 %2, %16 offset:16\n\t"
                   "ds_read_b128 %3, %17\n\tds_read_b128 %4, %17 offset:1024\n\tds_read_b128 %5, %17 offset:2048\n\tds_read_b128 %6, %17 offset:3072\n\t"
                   "ds_read_b128 %7, %17 offset:4096\n\tds_read_b128 %8, %17 offset:5120\n\tds_read_b128 %9, %17 offset:6144\n\tds_read_b128 %10, %17 offset:7168\n\t"
                   "ds_read_b128 %11, %17 offset:8192\n\tds_read_b128 %12, %17 offset:9216\n\tds_read_b128 %13, %17 offset:10240\n\tds_read_b128 %14, %17 offset:11264\n\t"
                   "s_waitcnt lgkmcnt(0)"
                   : "=&v"(f), "=&v"(a0), "=&v"(a1), "=&v"(v[0][0]), "=&v"(v[0][1]), "=&v"(v[0][2]), "=&v"(v[0][3]), "=&v"(v[1][0]), "=&v"(v[1][1]),
                     "=&v"(v[1][2]), "=&v"(v[1][3]), "=&v"(v[2][0]), "=&v"(v[2][1]), "=&v"(v[2][2]), "=&v"(v[2][3])
                   : "v"(fa), "v"(ma), "v"(xa) : "memory");
      if (__builtin_amdgcn_readfirstlane(f[0] & f[1] & f[2] & f[3])) break;
      ev_wait<4>(c, EV_XA);
    }
  }
  STAMP(1, 4);
  const float M = fmaxf(fmaxf(a0[0], a0[2]), fmaxf(a1[0], a1[2]));
  float wk[4] = {EX(a0[0] - M), EX(a0[2] - M), EX(a1[0] - M), EX(a1[2] - M)};
  const float inv = __builtin_amdgcn_rcpf((a0[1] * wk[0] + a0[3] * wk[1]) + (a1[1] * wk[2] + a1[3] * wk[3]));
#pragma unroll
  for (int k = 0; k < 4; ++k) wk[k] *= inv;
  u32x4 res[2];
#pragma unroll
  for (int g = 0; g < 4; ++g) {
    const f32x4 own = {o[3][4 * g], o[3][4 * g + 1], o[3][4 * g + 2], o[3][4 * g + 3]};
    const f32x4 f = (own * wk[3] + v[0][g] * wk[0]) + (v[1][g] * wk[1] + v[2][g] * wk[2]);
    res[g >> 1][(g & 1) * 2] = pk2(f[0], f[1]); res[g >> 1][(g & 1) * 2 + 1] = pk2(f[2], f[3]);
  }
  if (h) { res[1][2] = 0; res[1][3] = 0; }
#pragma unroll
  for (int s = 0; s < 2; ++s) st16s(s, res[s], c.rws, lane * 16, base_b(c, rb >> 1) + spread(POS_O + (unsigned)(((rb & 1) * 4 + w) * 16 + hd) * 2048u));
  STAMP(1, 5);
}
__device__ __forceinline__ void p2_publish(const Ctx& c) {
  publish<4>(c, EV_PUB2, flag_off(c, 1, (blockIdx.x >> 4) * 16 + (blockIdx.x & 7) * 2 + ((blockIdx.x >> 3) & 1), blockIdx.x >> 5));
}

__device__ __forceinline__ void stats_issue(const Ctx& c, int t, f32x2 (&v)[7]) {
  const unsigned vo = c.h * 1792 + c.r * 8, so = base_a(c, blockIdx.x & 7) + spread(POS_ST + (t & 7) * 4096u);
#pragma unroll
  for (int i = 0; i < 7; ++i)
    asm volatile("buffer_load_dwordx2 %0, %1, %2, %3 offen offset:%4 sc1" : "=v"(v[i]) : "v"(vo), "s"(c.rws), "s"(so), "i"(i * 256) : "memory");
}
#define STATS_WAIT(N, v) asm volatile("s_waitcnt vmcnt(" #N ")" : "+v"(v[0]), "+v"(v[1]), "+v"(v[2]), "+v"(v[3]), "+v"(v[4]), "+v"(v[5]), "+v"(v[6]) :: "memory")
__device__ __forceinline__ void stats_pin(f32x2 (&v)[7]) { asm volatile("" : "+v"(v[0]), "+v"(v[1]), "+v"(v[2]), "+v"(v[3]), "+v"(v[4]), "+v"(v[5]), "+v"(v[6])); }
__device__ __forceinline__ void stats_finish(const f32x2 (&v)[7], float& mu, float& rstd) {
  const f32x2 t = ((v[0] + v[1]) + (v[2] + v[3])) + ((v[4] + v[5]) + v[6]);
  mu = xhalf_sum(t[0]) * (1.f / 448.f);
  rstd = __builtin_amdgcn_rsqf(fmaxf(xhalf_sum(t[1]) * (1.f / 448.f) - mu * mu, 0.f) + EPS);
}
__device__ __forceinline__ unsigned relu2(float a, float b) {
  return __builtin_bit_cast(unsigned, __builtin_elementwise_max(__builtin_bit_cast(s16x2, pk2(a, b)), (s16x2){0, 0}));
}
constexpr int w1_slot(int st) { return st; }
constexpr int w2_pos(int k) { return k < 35 ? k : k - 35; }

__device__ __forceinline__ void pair_reduce(const Ctx& c, f32x16 (&acc)[2], int lbuf, int ev) {
  lptr dst = c.lds + lbuf + c.w * 4096 + c.lane * 16;
#pragma unroll
  for (int g = 0; g < 4; ++g) *(LAS f32x4*)(dst + g * 1024) = (f32x4){acc[1][4 * g], acc[1][4 * g + 1], acc[1][4 * g + 2], acc[1][4 * g + 3]};
  ev_set(c, ev, c.w);
  PIN(acc[0]);
  const unsigned fa = c.lbase + L_CNT + ev * 16 + (c.w ^ 2) * 4, sa = c.lbase + lbuf + (c.w ^ 2) * 4096 + c.lane * 16;
  f32x4 v0, v1, v2, v3;
#pragma unroll 1
  for (int spins = 0; spins < (1 << 24); ++spins) {
    unsigned f;
    asm volatile("ds_read_b32 %0, %5\n\tds_read_b128 %1, %6\n\tds_read_b128 %2, %6 offset:1024\n\tds_read_b128 %3, %6 offset:2048\n\t"
                 "ds_read_b128 %4, %6 offset:3072\n\ts_waitcnt lgkmcnt(0)"
                 : "=&v"(f), "=&v"(v0), "=&v"(v1), "=&v"(v2), "=&v"(v3) : "v"(fa), "v"(sa) : "memory");
    if (__builtin_amdgcn_readfirstlane(f)) break;
  }
#pragma unroll
  for (int e = 0; e < 4; ++e) { acc[0][e] += v0[e]; acc[0][4 + e] += v1[e]; acc[0][8 + e] += v2[e]; acc[0][12 + e] += v3[e]; }
}

__device__ __forceinline__ void p345_compute(const Ctx& c, const char* x, char* out, int rb3, int cb3, int rb4, int cb4) {
  const int lane = c.lane, w = c.w, r = c.r, h = c.h;
  lptr lds = c.lds;
  const rsrc_t rs = c.rws;
  const int wr = w & 1, kh = w >> 1, q = (lane & 15) >> 2;
  const int t3 = rb3 * 2 + wr, sub = cb3 * 2 + kh;
  const unsigned trl = ((lane >> 4) & 1) * 32 + (lane & 3) * 8, va = lane * 16;
  const unsigned grp = blockIdx.x & 7, ba = base_a(c, grp), bb_ = base_b(c, grp);
  u32x4 hres[2];
  {
    u32x4 bw[16][2];
    u32x2 xr[4];
    ev_wait<4>(c, EV_WO);
    const unsigned b0 = L_WO + (kh * 224 + 4 * h + q) * 128 + trl;
    unsigned bb[2] = {b0 + ((kh ^ (q >> 1)) * 64), b0 + ((kh ^ 1 ^ (q >> 1)) * 64)};
    PIN(bb[0]); PIN(bb[1]);
#pragma unroll
    for (int ks = 0; ks < 16; ++ks) {
      if (ks == 4) {
        SBAR(); p2_publish(c); STAMP(1, 6);
        const rsrc_t rx = mkrs(x, S_ * D_ * 2);
        const unsigned vx = (unsigned)(t3 * 32 + r) * 896u + h * 8 + sub * 64u;
#pragma unroll
        for (int g = 0; g < 4; ++g) asm volatile("buffer_load_dwordx2 %0, %1, %2, 0 offen offset:%3" : "=v"(xr[g]) : "v"(vx), "s"(rx), "i"(g * 16) : "memory");
      }
#pragma unroll
      for (int i = 0; i < 2; ++i) bw[ks][i] = ldstr2(lds + bb[i] + ((ks >> 1) * 28 + (ks & 1) * 16) * 128, 1024);
    }
    ev_set(c, EV_BPRE, w);
    ev_wait<4>(c, EV_BPRE);
    acquire(c, flag_off(c, 1, (rb3 >> 1) * 16, grp) + (lane & 15) * 256, EV_GO3);
    STAMP(2, 0);
    const unsigned sa = bb_ + 2u * (POS_O + (unsigned)((t3 & 7) * 32 + kh * 16) * 1024u);
    auto so = [&](int g) { return sa + (g & 1) * 4096u + (g >> 1) * 16384u; };
    u32x4 a[16];
    static_for<0, 10>([&](auto kc) { constexpr int k = decltype(kc)::value; ld16<(k & 3) * 1024>(a[k], rs, va, so(k >> 2)); });
    asm volatile("s_waitcnt vmcnt(10)" : "+v"(xr[0]), "+v"(xr[1]), "+v"(xr[2]), "+v"(xr[3]) :: "memory");
    f32x16 acc[2];
#pragma unroll
    for (int g = 0; g < 4; ++g)
#pragma unroll
      for (int i = 0; i < 2; ++i) { acc[0][4 * g + 2 * i] = bflo(xr[g][i]); acc[0][4 * g + 2 * i + 1] = bfhi(xr[g][i]); }
    acc[1] = f32x16{};
    pin16(acc[0]); pin16(acc[1]);
    static_for<0, 16>([&](auto kc) {
      constexpr int k = decltype(kc)::value;
      waitv<(k <= 6 ? 9 : 15 - k)>(a[k]);
      acc[1] = MFMA(frag(bw[k][1]), frag(a[k]), acc[1]);
      SBAR();
      if constexpr (k + 10 < 16) ld16<((k + 10) & 3) * 1024>(a[k + 10], rs, va, so((k + 10) >> 2));
      SBAR();
      acc[0] = MFMA(frag(bw[k][0]), frag(a[k]), acc[0]);
      SBAR();
    });
    STAMP(2, 7);
    pair_reduce(c, acc, L_XB, EV_RED3);
    STAMP(2, 8);
#pragma unroll
    for (int i = 0; i < 8; ++i) hres[i >> 2][i & 3] = pk2(acc[0][2 * i], acc[0][2 * i + 1]);
#pragma unroll
    for (int sx = 0; sx < 2; ++sx) st16s(sx, hres[sx], rs, va, bb_ + spread(POS_H + (unsigned)((t3 & 7) * 32 + sub * 2) * 1024u));
    f32x4 s4 = {}, q4 = {};
#pragma unroll
    for (int g = 0; g < 4; ++g) {
      const f32x4 v = {acc[0][4 * g], acc[0][4 * g + 1], acc[0][4 * g + 2], acc[0][4 * g + 3]};
      s4 += v; q4 += v * v;
    }
    const float s = xhalf_sum((s4[0] + s4[1]) + (s4[2] + s4[3])), sq = xhalf_sum((q4[0] + q4[1]) + (q4[2] + q4[3]));
    if (h == 0) __builtin_amdgcn_raw_buffer_store_b64((u32x2){__float_as_uint(s), __float_as_uint(sq)}, rs, r * 8, ba + spread(POS_ST + (t3 & 7) * 4096u) + sub * 256u, 16);
    STAMP(2, 9);
  }
  {
    const int t4 = rb4 * 4 + w;
    unsigned bo[4][3];
#pragma unroll
    for (int i = 0; i < 4; ++i) {
      bo[i][0] = (4 * h + q) * 256 + ((i ^ q) * 64) + trl; bo[i][1] = bo[i][0] + 65536; bo[i][2] = bo[i][0] + 131072;
      PIN(bo[i][0]); PIN(bo[i][1]); PIN(bo[i][2]);
    }
    publish<4>(c, EV_PUB3, flag_off(c, 2, rb3 * 8 + cb3, grp));
    STAMP(2, 10);
    ev_wait<4>(c, EV_CS);
    u32x2 cf[4];
#pragma unroll
    for (int i = 0; i < 4; ++i) { const u32x4 t = colfrag(*(LAS float*)(lds + L_CS + i * 128 + r * 4), h); cf[i] = (u32x2){t[0], t[1]}; PIN(cf[i]); }
    u32x4 a[28], bq[4][2];
    auto bread = [&](auto kc) {
      constexpr int k = decltype(kc)::value, pass = k / 28, ks = k % 28, off = w1_slot(ks >> 2) * SLOT + (ks & 3) * 4096;
      bq[k & 3][0] = ldstr2(lds + bo[2 * pass][off >> 16] + (off & 65535), 2048);
      bq[k & 3][1] = ldstr2(lds + bo[2 * pass + 1][off >> 16] + (off & 65535), 2048);
    };
    bread(IC<0>{}); bread(IC<1>{}); bread(IC<2>{});
    SBAR();
    const int rot = p4_rot(cb4);
    unsigned sa4[7];
#pragma unroll
    for (int g = 0; g < 7; ++g) { sa4[g] = bb_ + 2u * (POS_H + (t4 & 7) * 32768u) + spread((g + rot >= 7 ? g + rot - 7 : g + rot) * 4096u); asm volatile("" : "+s"(sa4[g])); }
    const unsigned sg = ba + 2u * (POS_G + (unsigned)((t4 & 7) * 112 + cb4 * 8) * 1024u);
    { const int l = lane < 14 ? lane : 0; acquire(c, flag_off(c, 2, rb4 * 16, grp) + (l >= 7 ? l + 1 : l) * 256, EV_GO4); }
    STAMP(3, 0);
    auto sa = [&](int g) { return sa4[g]; };
    f32x2 sv[7];
    static_for<0, 12>([&](auto kc) { constexpr int k = decltype(kc)::value; ld16<(k & 3) * 1024>(a[k], rs, va, sa(k >> 2)); });
    f32x16 acc[4] = {};
    u32x4 og, rf;
    static_for<0, 56>([&](auto kc) {
      constexpr int k = decltype(kc)::value, pass = k / 28, ks = k % 28;
      if constexpr (k < 28) waitv<((k + 11 < 27 ? k + 11 : 27) - k + (k >= 9 && k <= 20 ? 7 : 0))>(a[k]);
      if constexpr (k == 28) {
#pragma unroll
        for (int i = 0; i < 4; ++i) acc[i] = MFMA(frag((u32x4){cf[i][0], cf[i][1], 0u, 0u}), frag(rf), acc[i]);
        SBAR();
      }
      acc[2 * pass] = MFMA(frag(bq[k & 3][0]), frag(a[ks]), acc[2 * pass]);
      SBAR();
      if constexpr (k + 12 < 28) ld16<((k + 12) & 3) * 1024>(a[k + 12], rs, va, sa((k + 12) >> 2));
      if constexpr (k == 8) stats_issue(c, t4, sv);
      if constexpr (k + 3 < 56) bread(IC<k + 3>{});
      SBAR();
      acc[2 * pass + 1] = MFMA(frag(bq[k & 3][1]), frag(a[ks]), acc[2 * pass + 1]);
      SBAR();
      if constexpr (k == 21) {
        stats_pin(sv);
        float t = sv[0][0];
#pragma unroll
        for (int i = 1; i < 7; ++i) { t += sv[i][0]; pinf(t); }
        rf = rowfrag(xhalf_sum(t) * (-1.f / 448.f));
        pin4(rf);
        SBAR();
      }
      if constexpr (k == 24) { ev_set(c, EV_A4, w); SBAR(); }
      if constexpr (k >= 30 && k < 46) {
        constexpr int e = k - 30;
        og[e & 3] = relu2(acc[e >> 3][(e & 7) * 2], acc[e >> 3][(e & 7) * 2 + 1]);
        PIN(og);
        if constexpr ((e & 3) == 3) st16<(e >> 2) * 1024>(og, rs, va, sg);
        SBAR();
      }
    });
    ev_set(c, EV_X4, w);
    STAMP(3, 8);
    static_for<0, 4>([&](auto fc) {
      constexpr int f = decltype(fc)::value;
      u32x4 o;
#pragma unroll
      for (int e = 0; e < 4; ++e) o[e] = relu2(acc[2 + (f >> 1)][8 * (f & 1) + 2 * e], acc[2 + (f >> 1)][8 * (f & 1) + 2 * e + 1]);
      st16<f * 1024>(o, rs, va, sg + 4096u);
    });
    STAMP(3, 9);
    publish<4>(c, EV_PUB4, flag_off(c, 3, rb4 * 16 + cb4, grp));
    STAMP(3, 10);
  }
  {
    unsigned bo[2][3];
#pragma unroll
    for (int i = 0; i < 2; ++i) {
      bo[i][0] = kh * 2048 + (4 * h + q) * 128 + ((kh ^ i ^ (q >> 1)) * 64) + trl; bo[i][1] = bo[i][0] + 65536; bo[i][2] = bo[i][0] + 131072;
      PIN(bo[i][0]); PIN(bo[i][1]); PIN(bo[i][2]);
    }
    const int rot = p5_rot(cb3);
    const unsigned s5a = ba + 2u * (POS_G + (unsigned)((t3 & 7) * 112 + kh * 56 + rot * 4) * 1024u), s5b = rot ? s5a - 2u * 57344u : s5a;
    acquire(c, flag_off(c, 3, (rb3 >> 1) * 16, grp) + (lane < 14 ? lane : 0) * 256, EV_GO5);
    STAMP(4, 0);
    auto sa = [&](int g) { return (g >= 8 ? s5b : s5a) + spread(g * 4096u); };
    u32x4 a[W5], bq[4][2];
    f32x2 sv[7];
    static_for<0, W5>([&](auto kc) { constexpr int k = decltype(kc)::value; ld16<(k & 3) * 1024>(a[k], rs, va, sa(k >> 2)); });
    auto bread = [&](auto kc) {
      constexpr int k = decltype(kc)::value, off = w2_pos(k) * 4096;
      bq[k & 3][0] = ldstr2(lds + bo[0][off >> 16] + (off & 65535), 1024);
      bq[k & 3][1] = ldstr2(lds + bo[1][off >> 16] + (off & 65535), 1024);
    };
    ev_wait_half(c, EV_W2A);
    bread(IC<0>{}); bread(IC<1>{}); bread(IC<2>{});
    f32x16 acc[2] = {};
    static_for<0, 56>([&](auto kc) {
      constexpr int k = decltype(kc)::value;
      waitv<(k <= 56 - W5 ? W5 - 1 : 55 - k + 7)>(a[k % W5]);
      acc[1] = MFMA(frag(bq[k & 3][1]), frag(a[k % W5]), acc[1]);
      SBAR();
      if constexpr (k == 4 || k == 11 || k == 18) ev_set(c, EV_SEG0 + (k - 4) / 7, w);
      if constexpr (k == 32) { pin16(acc[0]); pin16(acc[1]); ev_wait_half(c, EV_W2B); }
      if constexpr (k + 3 < 56) bread(IC<k + 3>{});
      SBAR();
      acc[0] = MFMA(frag(bq[k & 3][0]), frag(a[k % W5]), acc[0]);
      SBAR();
      if constexpr (k + W5 < 56) ld16<((k + W5) & 3) * 1024>(a[k % W5], rs, va, sa((k + W5) >> 2));
      if constexpr (k == 56 - W5) stats_issue(c, t3, sv);
      SBAR();
    });
    STAMP(4, 15);
    float mu, rstd;
    STATS_WAIT(0, sv);
    stats_finish(sv, mu, rstd);
    pair_reduce(c, acc, L_XB, EV_RED5);
    STAMP(4, 16);
#pragma unroll
    for (int sx = 0; sx < 2; ++sx) {
      u32x4 o;
#pragma unroll
      for (int i = 0; i < 4; ++i) o[i] = pk2(acc[0][8 * sx + 2 * i] * rstd + bflo(hres[sx][i]), acc[0][8 * sx + 2 * i + 1] * rstd + bfhi(hres[sx][i]));
      auto x0 = __builtin_amdgcn_permlane32_swap(o[0], o[2], false, false);
      auto x1 = __builtin_amdgcn_permlane32_swap(o[1], o[3], false, false);
      *(u32x4*)(out + (unsigned)(t3 * 32 + r) * 896u + (sub * 32 + 16 * sx + 8 * h) * 2) = (u32x4){x0[0], x1[0], x0[1], x1[1]};
    }
    STAMP(4, 18);
  }
}

__device__ __forceinline__ void probe(const Ctx& c, int id, unsigned off) {
  unsigned best[2] = {~0u, ~0u};
#pragma unroll 1
  for (int i = 0; i < 10; ++i) {
    unsigned long long t0, t1;
    const unsigned z = 0;
    asm volatile("s_memtime %0\n\ts_waitcnt lgkmcnt(0)\n\tbuffer_store_dword %2, off, %3, %4 sc1\n\ts_waitcnt vmcnt(0)\n\ts_memtime %1\n\ts_waitcnt lgkmcnt(0)"
                 : "=&s"(t0), "=&s"(t1) : "v"(z), "s"(c.rws), "s"(off + (i & 1) * 8192u) : "memory");
    const unsigned d = (unsigned)(t1 - t0);
    if (i & 1) best[1] = min(best[1], d); else best[0] = min(best[0], d);
  }
  if (c.lane == 0) __builtin_amdgcn_raw_buffer_store_b64((u32x2){best[1] < best[0] ? 1u : 0u, c.tag}, c.rws, id * 8, OFF_PR, 16);
}
__device__ __forceinline__ void fetch_map(const Ctx& c) {
  const unsigned vo = (c.lane & 15) * 8;
  u32x2 g;
#pragma unroll 1
  for (int spins = 0; spins < (1 << 17); ++spins) {
    asm volatile("buffer_load_dwordx2 %0, %1, %2, %3 offen sc1\n\ts_waitcnt vmcnt(0)" : "=v"(g) : "v"(vo), "s"(c.rws), "s"(OFF_PR) : "memory");
    if (__all(g[1] == c.tag)) break;
    __builtin_amdgcn_s_sleep(8);
  }
  ev_set(c, EV_MAP, 0, ((unsigned)__ballot(g[0] & 1) & 0xFFFFu) | 0x80000000u);
}

__device__ __forceinline__ void helper(const Ctx& c, const char* wo, const char* w1, const char* w2, int cb3, int cb4) {
  const int lane = c.lane, w = c.w;
  const rsrc_t rwo = mkrs(wo, D_ * D_ * 2), rw1 = mkrs(w1, D_ * DFF * 2), rw2 = mkrs(w2, D_ * DFF * 2);
  const unsigned vw1 = (unsigned)(lane >> 4) * 3584u + (unsigned)((lane & 15) ^ ((lane >> 4) << 2)) * 16u;
  const unsigned vw2 = (unsigned)(lane >> 3) * 896u + (unsigned)((lane & 7) ^ (((lane >> 4) & 1) << 2)) * 16u;
  const int rot5 = p5_rot(cb3) * 4;
  const unsigned sw0 = (unsigned)((w >> 1) * 896 + (w & 1) * 8 + rot5 * 16) * 896u + cb3 * 128u;
  auto sw = [&](int k) { return sw0 + k * 14336u - (k + rot5 >= 56 ? 56u * 14336u : 0u); };
  u32x4 s1[16], st[49];
  auto ldw = [&](auto ic) { constexpr int i = decltype(ic)::value, k = i < 28 ? i : i + 7; ld16p(st[i], rw2, vw2, sw(k)); };
  ev_wait<1, 4>(c, EV_GO2);
  __builtin_amdgcn_s_sleep(32);
#pragma unroll 1
  for (int p = w; p < 57; p += 4) { dma16(rwo, vw2, p * 7168u + cb3 * 128u, c.lbase + L_WO + p * 1024); __builtin_amdgcn_s_sleep(PACE); }
#pragma unroll 1
  for (int p = w; p < 48; p += 4) { dma16(rw1, vw1, w1_piece(p, cb4), c.lbase + p * 1024); __builtin_amdgcn_s_sleep(PACE); }
  static_for<0, 16>([&](auto ic) { constexpr int i = decltype(ic)::value; ld16p(s1[i], rw1, vw1, w1_piece(48 + i * 4 + w, cb4)); __builtin_amdgcn_s_sleep(PACE); });
  static_for<0, 42>([&](auto ic) {
    ldw(ic);
    __builtin_amdgcn_s_sleep(PACE);
    if constexpr (decltype(ic)::value == 15) { waitv<16>(s1[15]); ev_set(c, EV_WO, w); }
  });
  STAMP(1, 16);
  if (w == 0) fetch_map(c);
  unsigned wb[3] = {(unsigned)(w * 1024 + lane * 16), 0u, 0u};
  wb[1] = wb[0] + 65536; wb[2] = wb[0] + 131072;
  PIN(wb[0]); PIN(wb[1]); PIN(wb[2]);
  ev_wait<4, 1>(c, EV_BPRE);
  static_for<0, 16>([&](auto ic) {
    constexpr int i = decltype(ic)::value, off = 3 * SLOT + i * 4096;
    waitv<42>(s1[i]);
    *(LAS u32x4*)(c.lds + wb[off >> 16] + (off & 65535)) = s1[i];
  });
  ev_set(c, EV_W1, w);
  ev_wait<4>(c, EV_W1);
  {
    const int q = (lane & 15) >> 2;
    lptr bp = c.lds + (4 * c.h + q) * 256 + ((w ^ q) * 64) + ((lane >> 4) & 1) * 32 + (lane & 3) * 8;
    float cs[4] = {0.f, 0.f, 0.f, 0.f};
#pragma unroll 1
    for (int sl = 0; sl < 2; ++sl) {
      u32x2 b[28];
#pragma unroll
      for (int k = 0; k < 28; ++k) b[k] = ldstr(bp + sl * 57344 + k * 2048);
      SBAR();
#pragma unroll
      for (int k = 0; k < 28; ++k) { cs[(k & 1) * 2] = dot2(b[k][0], ONES2, cs[(k & 1) * 2]); cs[(k & 1) * 2 + 1] = dot2(b[k][1], ONES2, cs[(k & 1) * 2 + 1]); }
    }
    *(LAS float*)(c.lds + L_CS + w * 128 + c.r * 4) = xhalf_sum((cs[0] + cs[1]) + (cs[2] + cs[3]));
    ev_set(c, EV_CS, w);
  }
  ev_wait<4, 2>(c, EV_A4);
#pragma unroll 1
  for (int k = 28; k < 35; ++k) dma16(rw2, vw2, sw(k), c.lbase + k * 4096 + w * 1024);
  static_for<42, 49>(ldw);
  auto stw = [&](auto ic) {
    constexpr int i = decltype(ic)::value, k = i < 28 ? i : i + 7, off = w2_pos(k) * 4096;
    waitv<0>(st[i]);
    *(LAS u32x4*)(c.lds + wb[off >> 16] + (off & 65535)) = st[i];
  };
  ev_wait<4, 2>(c, EV_X4);
  static_for<0, 28>(stw);
  ev_set(c, EV_W2A, w);
  ev_wait_half<2>(c, EV_SEG0);
  static_for<28, 35>(stw);
  ev_wait_half<2>(c, EV_SEG1);
  static_for<35, 42>(stw);
  ev_wait_half<2>(c, EV_SEG2);
  static_for<42, 49>(stw);
  ev_set(c, EV_W2B, w);
}

__global__ __launch_bounds__(512, 2) void block_fwd(const char* __restrict__ x, const char* __restrict__ wqkv, const char* __restrict__ wo,
                                                    const char* __restrict__ w1, const char* __restrict__ w2,
                                                    char* __restrict__ ws, char* __restrict__ out) {
  extern __shared__ __attribute__((aligned(16))) char smem[];
  unsigned long long pc;
  asm volatile("s_getpc_b64 %0" : "=s"(pc));
  asm volatile("" :: "s"(x), "s"(wqkv), "s"(wo), "s"(w1), "s"(w2), "s"(ws), "s"(out));
  Ctx c;
  c.lds = (lptr)smem; c.lbase = (unsigned)(uintptr_t)smem; c.ws = ws;
  c.wb = (char*)(((uintptr_t)ws + 0x1FFFFFull) & ~0x1FFFFFull);
  c.map = 0;
  c.rws = mkrs(c.wb, WS_USED);
  c.lane = threadIdx.x & 63; c.w8 = __builtin_amdgcn_readfirstlane(threadIdx.x >> 6); c.w = c.w8 & 3;
  c.r = c.lane & 31; c.h = c.lane >> 5;
  const int xcd = blockIdx.x & 7, j = blockIdx.x >> 3;
  const int rb1 = (xcd >> 1) * 4 + (j & 3), cb1 = (xcd & 1) * 8 + (j >> 2);
  const int rb3 = xcd * 4 + (j & 3), cb3 = j >> 2, rb4 = xcd * 2 + (j & 1), cb4 = j >> 1;
  c.tag = 0;
  if (c.w8 >= 4) {
    STAMP(0, 0);
    p1_load(c, x, wqkv, wo, w1, w2, rb1, cb1);
    c.tag = __builtin_amdgcn_readfirstlane(*(volatile LAS unsigned*)(c.lds + L_CNT + EV_TAG * 16));
    if (j < 28) helper(c, wo, w1, w2, cb3, cb4);
    else {
      ev_wait<1, 4>(c, EV_GO2);
      __builtin_amdgcn_s_sleep(48);
      if (c.w == 1 && j == 28) probe(c, xcd, OFF_A + (xcd << 21) + 0x1F0000u);
      if (c.w == 1 && j == 29) probe(c, 8 + xcd, OFF_B + (xcd << 20) + spread(POS_H + 28672u));
      if (c.w == 0) { __builtin_amdgcn_s_sleep(100); fetch_map(c); }
    }
    return;
  }
  if (threadIdx.x < EV_COUNT * 4) *(LAS unsigned*)(c.lds + L_CNT + threadIdx.x * 4) = 0;
  {
    const void* tp = j < 5 ? (const void*)c.wb : (j == 5 ? (const void*)out : (j == 6 ? (const void*)wo : (j == 7 ? (const void*)w1 : (j == 8 ? (const void*)w2 : (const void*)(pc & ~255ull)))));
    const unsigned tb = c.w8 != 3 ? 0u : (j < 5 ? WS_USED : (j == 5 ? S_ * D_ * 2 : (j == 6 ? D_ * D_ * 2 : (j < 9 ? D_ * DFF * 2 : CODE_BYTES))));
    c.rwarm = mkrs(tp, tb);
    c.vwarm = j < 9 ? c.lane * (j < 5 ? 131072u : 65536u) : (c.lane < 20 ? c.lane * 128u : OOB);
    c.swarm = j < 5 ? j * (64u * 131072u) : (j < 9 ? 0u : (j - 9) * 2560u);
  }
  if (cb1 < 10) p1_compute<3>(c, x, rb1, cb1);
  else if (cb1 == 10) p1_compute<2>(c, x, rb1, cb1);
  else p1_compute<0>(c, x, rb1, cb1);
  publish<4>(c, EV_PUB1, flag1_off(cb1 * 16 + rb1));
  ev_set(c, EV_TAG, 0, c.tag);
  ev_set(c, EV_P1D, c.w);
  STAMP(0, 10);
  p2_attn(c);
  if (j >= 28) {
    p2_publish(c);
    if (blockIdx.x == 255 && c.w8 == 0) {
#pragma unroll 1
      for (int i = 0; i < 4; ++i) poll(c, flag1_off(i * 64 + c.lane));
      if (c.lane == 0) __builtin_amdgcn_raw_buffer_store_b32(c.tag, c.rws, 0, OFF_EP, 16);
    }
    return;
  }
  p345_compute(c, x, out, rb3, cb3, rb4, cb4);
}

extern "C" void kernel_launch(void* const* d_in, const int* in_sizes, int n_in, void* d_out, int out_size,
                              void* d_ws, size_t ws_size, hipStream_t stream) {
  (void)hipFuncSetAttribute((const void*)block_fwd, hipFuncAttributeMaxDynamicSharedMemorySize, LDS_BYTES);
  block_fwd<<<256, 512, LDS_BYTES, stream>>>((const char*)d_in[0], (const char*)d_in[1], (const char*)d_in[2],
                                             (const char*)d_in[4], (const char*)d_in[5], (char*)d_ws, (char*)d_out);
}
```

```cpp
#include <hip/hip_runtime.h>
#include <hip/hip_bf16.h>
#include <stdint.h>

typedef __attribute__((ext_vector_type(8))) short bf16x8;
typedef __attribute__((ext_vector_type(4))) short s16x4;
typedef __attribute__((ext_vector_type(2))) short s16x2;
typedef __attribute__((ext_vector_type(2))) __bf16 bf16x2;
typedef __attribute__((ext_vector_type(16))) float f32x16;
typedef __attribute__((ext_vector_type(8))) __bf16 bf16x8n;
typedef __attribute__((ext_vector_type(16))) __bf16 bf16x16n;
typedef __attribute__((ext_vector_type(8))) float f32x8;
typedef __attribute__((ext_vector_type(4))) float f32x4;
typedef __attribute__((ext_vector_type(2))) float f32x2;
typedef __attribute__((ext_vector_type(4))) unsigned u32x4;
typedef __attribute__((ext_vector_type(2))) unsigned u32x2;
typedef __amdgpu_buffer_rsrc_t rsrc_t;
#define LAS __attribute__((address_space(3)))
typedef LAS char* lptr;

constexpr int S_ = 2048, D_ = 448, DFF = 1792;
constexpr unsigned QKV_BYTES = 16u * 64u * 2048u;
constexpr unsigned OFF_Q = 0, OFF_K = QKV_BYTES, OFF_V = 2 * QKV_BYTES;
constexpr unsigned OFF_F1 = 3 * QKV_BYTES;
constexpr unsigned OFF_EP = OFF_F1 + 65536u;
constexpr unsigned OFF_PR = OFF_EP + 256u;
constexpr unsigned OFF_LN = OFF_PR + 256u;
constexpr unsigned OFF_A = 8u << 20, OFF_B = 24u << 20, WS_USED = 32u << 20;
constexpr unsigned POS_G = 0, POS_ST = 8u * 112u * 1024u, POS_FL = POS_ST + 8u * 4096u;
constexpr unsigned POS_O = 0, POS_H = 8u * 32768u;
constexpr unsigned OOB = 0x7F000000u;
constexpr int SLOT = 16384, L_XA = 3 * SLOT, L_WO = 6 * SLOT, L_ML = L_WO + 57 * 1024, L_XB = 35 * 4096, LDS_BYTES = 163840;
constexpr int L_CS = L_ML + 4096, L_ROW = L_CS + 1536, L_CNT = LDS_BYTES - 1024, L_DUMMY = L_CNT + 512;
constexpr int P1_SLOT = 16384 + 12288;
enum { EV_PUB1, EV_PUB2, EV_PUB3, EV_PUB4, EV_GO2, EV_GO3, EV_GO4, EV_GO5, EV_XA, EV_WO, EV_BPRE, EV_W1, EV_CS,
       EV_P1D, EV_TAG, EV_MAP, EV_RED3, EV_A4, EV_X4, EV_W2A, EV_SEG0, EV_SEG1, EV_SEG2, EV_W2B, EV_RED5, EV_COUNT };

constexpr unsigned CODE_BYTES = 38400;
#define PACE 2
#define W5 14
constexpr float QSCALE = 0.18898223650461362f * 1.4426950408889634f;
constexpr float EPS = 1e-5f;
constexpr unsigned ONES2 = 0x3F803F80u;

#define MFMA(a, b, c) __builtin_amdgcn_mfma_f32_32x32x16_bf16((a), (b), (c), 0, 0, 0)
#define WAITV_BAR(N) asm volatile("s_waitcnt vmcnt(" #N ")\n\ts_barrier" ::: "memory")
#define BAR() asm volatile("s_waitcnt lgkmcnt(0)\n\ts_barrier" ::: "memory")
#define SBAR() __builtin_amdgcn_sched_barrier(0)
#define PIN(x) asm volatile("" : "+v"(x))
#define EX(v) __builtin_amdgcn_exp2f(v)
#define STAMP(kid, i) do {} while (0)

__device__ __forceinline__ rsrc_t mkrs(const void* p, unsigned bytes) {
  return __builtin_amdgcn_make_buffer_rsrc((void*)p, (short)0, (int)bytes, 0x00020000);
}
__device__ __forceinline__ void dma16(rsrc_t r, unsigned voff, unsigned soff, unsigned ldsaddr) {
  asm volatile("s_mov_b32 m0, %0\n\ts_nop 0\n\tbuffer_load_dwordx4 %1, %2, %3 offen lds"
               :: "s"(ldsaddr), "v"(voff), "s"(r), "s"(soff) : "memory");
}
__device__ __forceinline__ void dma16x4_sc1(rsrc_t r, unsigned voff, unsigned soff, unsigned ldsaddr) {
  asm volatile("s_mov_b32 m0, %0\n\ts_nop 0\n\tbuffer_load_dwordx4 %1, %2, %3 offen sc1 lds\n\t"
               "buffer_load_dwordx4 %1, %2, %3 offen offset:1024 sc1 lds\n\t"
               "buffer_load_dwordx4 %1, %2, %3 offen offset:2048 sc1 lds\n\t"
               "buffer_load_dwordx4 %1, %2, %3 offen offset:3072 sc1 lds"
               :: "s"(ldsaddr), "v"(voff), "s"(r), "s"(soff) : "memory");
}
template <int IMM>
__device__ __forceinline__ void ld16(u32x4& d, rsrc_t r, unsigned voff, unsigned soff) {
  asm volatile("buffer_load_dwordx4 %0, %1, %2, %3 offen offset:%4 sc1" : "=v"(d) : "v"(voff), "s"(r), "s"(soff), "i"(IMM) : "memory");
}
__device__ __forceinline__ void ld16p(u32x4& d, rsrc_t r, unsigned voff, unsigned soff) {
  asm volatile("buffer_load_dwordx4 %0, %1, %2, %3 offen" : "=v"(d) : "v"(voff), "s"(r), "s"(soff) : "memory");
}
template <int N>
__device__ __forceinline__ void waitv(u32x4& x) { asm volatile("s_waitcnt vmcnt(%1)" : "+v"(x) : "i"(N) : "memory"); }
template <int IMM>
__device__ __forceinline__ void st16(u32x4 v, rsrc_t r, unsigned voff, unsigned soff) {
  asm volatile("buffer_store_dwordx4 %0, %1, %2, %3 offen offset:%4 sc1\n\ts_nop 1" :: "v"(v), "v"(voff), "s"(r), "s"(soff), "i"(IMM) : "memory");
}
__device__ __forceinline__ void st16s(int s, u32x4 v, rsrc_t r, unsigned voff, unsigned soff) {
  if (s) st16<1024>(v, r, voff, soff); else st16<0>(v, r, voff, soff);
}
__device__ __forceinline__ unsigned pk2(float a, float b) {
  f32x2 v = {a, b};
  return __builtin_bit_cast(unsigned, __builtin_convertvector(v, bf16x2));
}
__device__ __forceinline__ float bflo(unsigned u) { return __uint_as_float(u << 16); }
__device__ __forceinline__ float bfhi(unsigned u) { return __uint_as_float(u & 0xffff0000u); }
__device__ __forceinline__ float dot2(unsigned a, unsigned b, float c) {
  return __builtin_amdgcn_fdot2_f32_bf16(__builtin_bit_cast(bf16x2, a), __builtin_bit_cast(bf16x2, b), c, false);
}
__device__ __forceinline__ u32x4 lds128(lptr p) { return *(LAS u32x4*)p; }
__device__ __forceinline__ f32x4 ldsf4(lptr p) { return *(LAS f32x4*)p; }
__device__ __forceinline__ u32x2 ldstr(lptr p) {
  return __builtin_bit_cast(u32x2, __builtin_amdgcn_ds_read_tr16_b64_v4i16((LAS s16x4*)p));
}
__device__ __forceinline__ u32x4 ldstr2(lptr p, int d) {
  const u32x2 a = ldstr(p), b = ldstr(p + d);
  return (u32x4){a[0], a[1], b[0], b[1]};
}
__device__ __forceinline__ bf16x8 frag(u32x4 v) { return __builtin_bit_cast(bf16x8, v); }
__device__ __forceinline__ float xhalf_sum(float v) {
  auto r = __builtin_amdgcn_permlane32_swap(__float_as_uint(v), __float_as_uint(v), false, false);
  return __uint_as_float(r[0]) + __uint_as_float(r[1]);
}
__device__ __forceinline__ float xhalf_max(float v) {
  auto r = __builtin_amdgcn_permlane32_swap(__float_as_uint(v), __float_as_uint(v), false, false);
  return fmaxf(__uint_as_float(r[0]), __uint_as_float(r[1]));
}
__device__ __forceinline__ u32x4 colfrag(float cs, int h) {
  const unsigned a = pk2(cs, cs);
  const float lo = cs - bflo(a);
  const unsigned b = pk2(lo, lo);
  return (u32x4){h ? 0u : a, h ? 0u : b, 0u, 0u};
}
__device__ __forceinline__ u32x4 rowfrag(float nmu) {
  const unsigned a = pk2(nmu, nmu);
  const unsigned p = pk2(nmu, nmu - bflo(a));
  return (u32x4){p, p, 0u, 0u};
}

__device__ __forceinline__ void pin16(f32x16& x) { PIN(x); }
__device__ __forceinline__ void pin4(u32x4& x) { PIN(x); }
__device__ __forceinline__ void pinf(float& x) { PIN(x); }
template <int V> struct IC { static constexpr int value = V; };
template <int I, int N, class F>
__device__ __forceinline__ void static_for(F&& f) {
  if constexpr (I < N) { f(IC<I>{}); static_for<I + 1, N>(f); }
}

struct Ctx {
  lptr lds; unsigned lbase;
  char* ws;
  rsrc_t rws;
  unsigned tag, map;
  char* wb;
  int lane, w8, w, r, h;
};

__device__ __forceinline__ constexpr unsigned spread(unsigned L) { return L + (L & ~8191u); }
__device__ __forceinline__ unsigned base_a(const Ctx& c, unsigned g) { return OFF_A + (g << 21) + (((c.map >> g) & 1u) << 13); }
__device__ __forceinline__ unsigned base_b(const Ctx& c, unsigned g) { return OFF_B + (g << 20) + (((c.map >> (8 + g)) & 1u) << 13); }
__device__ __forceinline__ void stampf(const Ctx& c, int kid, int i) { STAMP(kid, i); }
__device__ __forceinline__ void ev_set(const Ctx& c, int idx, int word, unsigned one = 1) {
  const unsigned addr = c.lbase + L_CNT + idx * 16 + word * 4;
  asm volatile("s_mov_b64 exec, 1\n\tds_write_b32 %0, %1\n\ts_mov_b64 exec, -1" :: "v"(addr), "v"(one) : "memory");
}
template <int NWORDS, int SLEEP = 0>
__device__ __forceinline__ void ev_wait(const Ctx& c, int idx) {
  const unsigned addr = c.lbase + L_CNT + idx * 16;
#pragma unroll 1
  for (int spins = 0; spins < (1 << 24); ++spins) {
    u32x4 v;
    asm volatile("ds_read_b128 %0, %1\n\ts_waitcnt lgkmcnt(0)" : "=v"(v) : "v"(addr) : "memory");
    if (__builtin_amdgcn_readfirstlane(NWORDS == 4 ? (v[0] & v[1] & v[2] & v[3]) : v[0])) break;
    if (SLEEP) __builtin_amdgcn_s_sleep(SLEEP);
  }
}
template <int SLEEP = 0>
__device__ __forceinline__ void ev_wait_half(const Ctx& c, int idx) {
  const unsigned addr = c.lbase + L_CNT + idx * 16 + (c.w >> 1) * 8;
#pragma unroll 1
  for (int spins = 0; spins < (1 << 24); ++spins) {
    u32x2 v;
    asm volatile("ds_read_b64 %0, %1\n\ts_waitcnt lgkmcnt(0)" : "=v"(v) : "v"(addr) : "memory");
    if (__builtin_amdgcn_readfirstlane(v[0] & v[1])) break;
    if (SLEEP) __builtin_amdgcn_s_sleep(SLEEP);
  }
}
template <int NW>
__device__ __forceinline__ void publish(const Ctx& c, int idx, unsigned flag_off) {
  const unsigned addr = c.lbase + L_CNT + idx * 16, one = 1;
  unsigned old;
  asm volatile("s_waitcnt vmcnt(0)\n\ts_mov_b64 exec, 1\n\tds_add_rtn_u32 %0, %1, %2\n\ts_mov_b64 exec, -1\n\ts_waitcnt lgkmcnt(0)"
               : "=v"(old) : "v"(addr), "v"(one) : "memory");
  if (__builtin_amdgcn_readfirstlane(old) == NW - 1) {
    const unsigned tag = c.tag;
    asm volatile("s_mov_b64 exec, 1\n\tbuffer_store_dword %0, off, %1, %2 sc1\n\ts_mov_b64 exec, -1" :: "v"(tag), "s"(c.rws), "s"(flag_off) : "memory");
  }
}
__device__ __forceinline__ void poll(const Ctx& c, unsigned voff) {
#pragma unroll 1
  for (int spins = 0; spins < (1 << 17); ++spins) {
    unsigned f;
    asm volatile("buffer_load_dword %0, %1, %2, 0 offen sc1\n\ts_waitcnt vmcnt(0)" : "=v"(f) : "v"(voff), "s"(c.rws) : "memory");
    if (__all(f == c.tag)) break;
  }
}
__device__ __forceinline__ void acquire(const Ctx& c, unsigned voff, int ev) {
  if (c.w == 0) { poll(c, voff); ev_set(c, ev, 0); }
  else ev_wait<1>(c, ev);
}
__device__ __forceinline__ unsigned flag1_off(int idx) { return OFF_F1 + (unsigned)idx * 256u; }
__device__ __forceinline__ unsigned flag_off(const Ctx& c, int phase, int idx, unsigned grp) { return base_a(c, grp) + spread(POS_FL + (phase - 1) * 8192u) + (idx & 31) * 256u; }
__device__ __forceinline__ void touch_lds(const Ctx& c, rsrc_t r, unsigned voff, unsigned soff) {
  asm volatile("s_mov_b32 m0, %0\n\ts_nop 0\n\tbuffer_load_dword %1, %2, %3 offen lds" :: "s"(c.lbase + L_DUMMY), "v"(voff), "s"(r), "s"(soff) : "memory");
}

template <int CTRL>
__device__ __forceinline__ float dpp_add(float v) {
  return v + __int_as_float(__builtin_amdgcn_update_dpp(0, __float_as_int(v), CTRL, 0xf, 0xf, true));
}
__device__ __forceinline__ float half_sum(float v) {
  v = dpp_add<0x140>(dpp_add<0x141>(dpp_add<0x4E>(dpp_add<0xB1>(v))));
  auto r = __builtin_amdgcn_permlane16_swap(__float_as_uint(v), __float_as_uint(v), false, false);
  return __uint_as_float(r[0]) + __uint_as_float(r[1]);
}

__device__ __forceinline__ void p1_stats(Ctx& c, const char* x, const char* wqkv, const char* wo, const char* w1, const char* w2, int rb, int cb) {
  const int w = c.w, r = c.r;
  const int row = rb * 128 + cb * 8 + w * 2 + c.h;
  u32x4 xs0, xs1;
  const unsigned vo = r < 28 ? (unsigned)row * 896u + r * 32u : OOB;
  ld16p(xs0, mkrs(x, S_ * D_ * 2), vo, 0); ld16p(xs1, mkrs(x, S_ * D_ * 2), vo, 16);
  const unsigned share = w == 0 ? 1568u : (w == 3 ? 4704u : 6272u);
  touch_lds(c, mkrs(w == 0 ? wo : (w == 1 ? w1 : (w == 2 ? w2 : wqkv)), share * 256u), c.lane * 128u < share + 64u ? c.lane * 128u : OOB, blockIdx.x * share);
  {
    const unsigned b = blockIdx.x, g = b & 7, j = b >> 3;
    const unsigned fl = OFF_A + ((w == 1 ? b >> 5 : g) << 21) + spread(POS_FL + (w - 1) * 8192u) +
                        (w == 1 ? ((b >> 4) & 1) * 16 + g * 2 + (j & 1) : (w == 2 ? (j & 3) * 8 + (j >> 2) : (j & 1) * 16 + (j >> 1))) * 256u;
    const unsigned a = w ? fl + c.lane * 8192u : (c.lane ? OFF_LN + (rb * 128 + cb * 8) * 16u : flag1_off(cb * 16 + rb));
    touch_lds(c, c.rws, c.lane < 2 ? a : OOB, 0);
  }
  if (threadIdx.x < EV_COUNT * 4) *(LAS unsigned*)(c.lds + L_CNT + threadIdx.x * 4) = 0;
  unsigned prev;
  asm volatile("s_load_dword %0, %1, 0x0\n\ts_waitcnt lgkmcnt(0)" : "=s"(prev) : "s"(c.wb + OFF_EP) : "memory");
  c.tag = prev * 0x9E3779B1u + 0x7F4A7C15u;
  asm volatile("s_waitcnt vmcnt(2)" : "+v"(xs0), "+v"(xs1) :: "memory");
  float s0 = 0.f, s1 = 0.f, q0 = 0.f, q1 = 0.f;
#pragma unroll
  for (int e = 0; e < 4; ++e) { s0 = dot2(xs0[e], ONES2, s0); s1 = dot2(xs1[e], ONES2, s1); q0 = dot2(xs0[e], xs0[e], q0); q1 = dot2(xs1[e], xs1[e], q1); }
  const float mu = half_sum(s0 + s1) * (1.f / 448.f);
  const float rstd = __builtin_amdgcn_rsqf(fmaxf(half_sum(q0 + q1) * (1.f / 448.f) - mu * mu, 0.f) + EPS);
  st16<0>((u32x4){__float_as_uint(-mu), c.tag, __float_as_uint(rstd), c.tag}, c.rws, r == 0 ? (unsigned)row * 16u : OOB, OFF_LN);
}

template <int NQ>
__device__ __forceinline__ void p1_compute(const Ctx& c, int rb, int cb) {
  const int lane = c.lane, w = c.w, r = c.r, h = c.h;
  lptr lds = c.lds;
  const int arow = w * 32 + r;
  const unsigned aoff = arow * 128 + ((h ^ ((arow >> 1) & 7)) * 16);
  const unsigned boff = 16384 + (8 * h + ((lane & 15) >> 2)) * 192 + ((lane >> 4) & 1) * 32 + (lane & 3) * 8 + (cb & 1) * 8;
  unsigned ao[4], bo[4];
#pragma unroll
  for (int i = 0; i < 4; ++i) { const int ks = (w + i) & 3; ao[i] = aoff ^ (ks * 32); bo[i] = boff + ks * 3072; }
  f32x16 acc[3] = {};
  float cs[3] = {0.f, 0.f, 0.f};
  u32x4 a[4], b[4][3];
  auto dots = [&]() {
#pragma unroll
    for (int e = 0; e < 4; ++e)
#pragma unroll
      for (int n = 0; n < 3; ++n) cs[n] = dot2(b[0][n][e], ONES2, cs[n]);
    PIN(cs[0]); PIN(cs[1]); PIN(cs[2]);
    SBAR();
  };
  BAR();
  STAMP(0, 1);
#pragma unroll
  for (int i = 0; i < 4; ++i) {
#pragma unroll
    for (int n = 0; n < 3; ++n) b[i][n] = ldstr2(lds + bo[i] + n * 56, 768);
    a[i] = lds128(lds + ao[i]);
  }
  SBAR();
  u32x4 ln = {};
  const unsigned vln = (unsigned)(rb * 128 + arow) * 16u;
#pragma unroll 1
  for (int st = 1; st < 7; ++st) {
    lptr sb = lds + (st >= 5 ? st - 5 : st) * P1_SLOT;
    dots();
    asm volatile("s_barrier" ::: "memory");
#pragma unroll
    for (int i = 0; i < 4; ++i)
#pragma unroll
      for (int n = 0; n < 3; ++n) {
        acc[n] = n < NQ ? MFMA(frag(b[i][n]), frag(a[i]), acc[n]) : MFMA(frag(a[i]), frag(b[i][n]), acc[n]);
        SBAR();
        b[i][n] = ldstr2(sb + bo[i] + n * 56, 768);
        if (n == 2) a[i] = lds128(sb + ao[i]);
        if (i == 0 && n == 0) asm volatile("s_cmp_lg_u32 %4, 6\n\ts_cbranch_scc1 .Lnl%=\n\tbuffer_load_dwordx4 %0, %1, %2, %3 offen sc1\n.Lnl%=:" : "+v"(ln) : "v"(vln), "s"(c.rws), "s"(OFF_LN), "s"(st) : "memory", "scc");
        SBAR();
      }
  }
  STAMP(0, 7);
  dots();
#pragma unroll
  for (int i = 0; i < 2; ++i)
#pragma unroll
    for (int n = 0; n < 3; ++n) {
      acc[n] = n < NQ ? MFMA(frag(b[i][n]), frag(a[i]), acc[n]) : MFMA(frag(a[i]), frag(b[i][n]), acc[n]);
      if (i == 0) {
        const float t = xhalf_sum(cs[n]);
        *(LAS float*)(lds + L_CS + (n * 32 + r) * 16 + w * 4) = t;
      }
      SBAR();
    }
  BAR();
  f32x4 p4[3], r4[4];
  u32x4 cf[3];
#pragma unroll
  for (int n = 0; n < 3; ++n) p4[n] = ldsf4(lds + L_CS + (n * 32 + r) * 16);
  SBAR();
#pragma unroll
  for (int i = 2; i < 4; ++i)
#pragma unroll
    for (int n = 0; n < 3; ++n) {
      acc[n] = n < NQ ? MFMA(frag(b[i][n]), frag(a[i]), acc[n]) : MFMA(frag(a[i]), frag(b[i][n]), acc[n]);
      if (i == 3) {
        float t0 = p4[n][0] + p4[n][1], t1 = p4[n][2] + p4[n][3];
        PIN(t0); PIN(t1);
        cf[n] = colfrag(t0 + t1, h);
        PIN(cf[n]);
      }
      SBAR();
    }
  pin16(acc[0]); pin16(acc[1]); pin16(acc[2]);
  waitv<0>(ln);
#pragma unroll 1
  for (int spins = 0; spins < (1 << 17) && !__all(ln[1] == c.tag && ln[3] == c.tag); ++spins)
    asm volatile("buffer_load_dwordx4 %0, %1, %2, %3 offen sc1\n\ts_waitcnt vmcnt(0)" : "=v"(ln) : "v"(vln), "s"(c.rws), "s"(OFF_LN) : "memory");
  const float rstd = __uint_as_float(ln[2]);
  const u32x4 rf = rowfrag(__uint_as_float(ln[0]));
  if (NQ < 3) {
    lptr srow = lds + L_ROW + w * 128;
    *(LAS float*)(srow + r * 4) = rstd;
#pragma unroll
    for (int g = 0; g < 4; ++g) r4[g] = ldsf4(srow + (8 * g + 4 * h) * 4);
  }
#pragma unroll
  for (int n = 0; n < 3; ++n) acc[n] = n < NQ ? MFMA(frag(cf[n]), frag(rf), acc[n]) : MFMA(frag(rf), frag(cf[n]), acc[n]);
  STAMP(0, 8);
  const int t = rb * 4 + w;
  const unsigned vk = ((lane & 0x33) | ((lane & 4) << 1) | ((lane & 8) >> 1)) * 16;
#pragma unroll
  for (int n = 0; n < 3; ++n) {
    const int u = cb * 3 + n;
    const float sc = u < 16 ? rstd * QSCALE : rstd;
#pragma unroll
    for (int s = 0; s < 2; ++s) {
      u32x4 o;
#pragma unroll
      for (int i = 0; i < 4; ++i) {
        const int e = 8 * s + 2 * i;
        o[i] = n < NQ ? pk2(acc[n][e] * sc, acc[n][e + 1] * sc) : pk2(acc[n][e] * r4[e >> 2][e & 3], acc[n][e + 1] * r4[e >> 2][(e & 3) + 1]);
      }
      if (n < NQ) {
        if (s == 1 && h) { o[2] = u < 16 ? 0u : ONES2; o[3] = u < 16 ? 0u : 0x00003F80u; }
      } else if (r >= 28) {
        o = (u32x4){ONES2, ONES2, ONES2, ONES2};
      }
      st16s(s, o, c.rws, (n < NQ && u >= 16) ? vk : lane * 16, (unsigned)(u * 64 + t) * 2048u);
    }
  }
  STAMP(0, 9);
}

__device__ __forceinline__ void p1_load(const Ctx& c, const char* x, const char* wqkv, int rb, int cb) {
  const int lane = c.lane, w = c.w;
  const rsrc_t rx = mkrs(x, S_ * D_ * 2), rw = mkrs(wqkv, D_ * 3 * D_ * 2);
  unsigned vxa[2], vw[3];
#pragma unroll
  for (int p = 0; p < 2; ++p) vxa[p] = (unsigned)(lane >> 3) * 896u + (unsigned)((lane & 7) ^ ((4 * p + (lane >> 4)) & 7)) * 16u;
#pragma unroll
  for (int q = 0; q < 3; ++q) { const int i = q * 64 + lane; vw[q] = (unsigned)(i / 12) * 2688u + (unsigned)(i % 12) * 16u; }
  const unsigned sx0 = (unsigned)(rb * 128 + w * 32) * 896u, sw0 = (((unsigned)cb * 168u) & ~15u) + (unsigned)w * (16u * 2688u);
#pragma unroll 1
  for (int st = 0; st < 7; ++st) {
    const unsigned lb = c.lbase + (st >= 5 ? st - 5 : st) * P1_SLOT;
#pragma unroll
    for (int p = 0; p < 4; ++p) dma16(rx, vxa[p & 1], sx0 + st * 128u + p * 7168u, lb + (w * 4 + p) * 1024);
#pragma unroll
    for (int q = 0; q < 3; ++q) dma16(rw, vw[q], sw0 + st * (64u * 2688u), lb + 16384 + (w * 3 + q) * 1024);
    if (st) WAITV_BAR(7);
  }
  WAITV_BAR(0);
  BAR();
}

#define SMF(v, p, c) __builtin_amdgcn_smfmac_f32_32x32x32_bf16(__builtin_bit_cast(bf16x8n, v), __builtin_bit_cast(bf16x16n, p), (c), sidx, 0, 0)
#define LO8(S) __builtin_shufflevector(S, S, 0, 1, 2, 3, 4, 5, 6, 7)
#define ATT_UNIT(SC, SN, KN0, KN1, QN, OPREV, VPREV1, OCUR, VCUR0, HOOK_A, HOOK_B, HOOK_C, HOOK_D)       \
  do {                                                                                                     \
    SN = MFMA(frag(KN0), frag(QN[0]), zero); SBAR();                                                       \
    SC[0] = EX(SC[0]); SC[1] = EX(SC[1]); SC[2] = EX(SC[2]); SC[3] = EX(SC[3]);                            \
    PIN(SC); SBAR(); HOOK_A;                                                                               \
    SN = MFMA(frag(KN1), frag(QN[1]), SN); SBAR();                                                         \
    SC[4] = EX(SC[4]); SC[5] = EX(SC[5]); SC[6] = EX(SC[6]); SC[7] = EX(SC[7]);                            \
    PIN(SC); SBAR(); HOOK_B;                                                                               \
    OPREV = SMF(VPREV1, e, OPREV); SBAR();                                                                 \
    e[0] = EX(SC[8]); e[1] = EX(SC[9]); e[2] = EX(SC[10]); e[3] = EX(SC[11]);                              \
    PIN(e); SBAR(); HOOK_C;                                                                                \
    OCUR = SMF(VCUR0, LO8(SC), OCUR); SBAR();                                                              \
    e[4] = EX(SC[12]); e[5] = EX(SC[13]); e[6] = EX(SC[14]); e[7] = EX(SC[15]);                            \
    PIN(e); SBAR(); HOOK_D;                                                                                \
  } while (0)

__device__ __forceinline__ int p4_rot(int cb4) { return cb4 >= 7 ? cb4 - 7 : cb4; }
#define P5ROT (cb3 & 1) * 6
__device__ __forceinline__ int p5_rot(int cb3) { return P5ROT; }
__device__ __forceinline__ unsigned w1_piece(int p, int cb4) {
  const int q = p + p4_rot(cb4) * 16;
  return (unsigned)(q >= 112 ? q - 112 : q) * 14336u + cb4 * 256u;
}
template <bool EXACT>
__device__ __forceinline__ void attn_pass(const Ctx& c, f32x16 (&o)[4], float (&m)[4]) {
  const int lane = c.lane, w = c.w, h = c.h;
  const int hd = (blockIdx.x & 7) * 2 + ((blockIdx.x >> 3) & 1), rb = blockIdx.x >> 4;
  const unsigned vo = lane * 16;
  const unsigned sq = OFF_Q + (unsigned)(hd * 64 + rb * 4) * 2048u;
  const unsigned sk = OFF_K + (unsigned)(hd * 64 + w * 16) * 2048u, sv = sk + QKV_BYTES;
  const unsigned rot = (unsigned)(rb & 7) * 4096u, rot1 = (rot + 4096u) & 32767u;
  const rsrc_t rs = c.rws;
  const f32x16 zero = {};
  u32x4 q0[2], q1[2], q2[2], q3[2], ka0, ka1, va0, va1, kb0, kb1, vb0, vb1, kc0, kc1, vc0, vc1, kd0, kd1, vd0, vd1;
  unsigned sqb[4];
#pragma unroll
  for (int b = 0; b < 4; ++b) { m[b] = -INFINITY; sqb[b] = sq + ((w + 1 + b) & 3) * 2048u; }
  f32x8 e;
  int sidx = 0xDDDD;
  PIN(sidx);
#pragma unroll 1
  for (int i = EXACT ? 15 : 0; i >= 0; --i) {
    const unsigned ko = sk + ((rot + i * 2048u) & 32767u);
    ld16<0>(ka0, rs, vo, ko); ld16<0>(q0[0], rs, vo, sqb[0]); ld16<1024>(ka1, rs, vo, ko); ld16<1024>(q0[1], rs, vo, sqb[0]);
    ld16<0>(q1[0], rs, vo, sqb[1]); ld16<1024>(q1[1], rs, vo, sqb[1]); ld16<0>(q2[0], rs, vo, sqb[2]); ld16<1024>(q2[1], rs, vo, sqb[2]);
    ld16<0>(q3[0], rs, vo, sqb[3]); ld16<1024>(q3[1], rs, vo, sqb[3]);
    ld16<0>(va0, rs, vo, sv + rot);
    ld16<1024>(va1, rs, vo, sv + rot); ld16<2048>(kb0, rs, vo, sk + rot); ld16<3072>(kb1, rs, vo, sk + rot); ld16<2048>(vb0, rs, vo, sv + rot);
#pragma unroll
    for (int b = 0; b < 4; ++b) { o[b] = zero; pin16(o[b]); }
    vd1 = (u32x4){}; e = f32x8{};
    PIN(vd1); PIN(e);
    asm volatile("s_waitcnt vmcnt(5)" : "+v"(q0[0]), "+v"(q0[1]), "+v"(q1[0]), "+v"(q1[1]), "+v"(q2[0]), "+v"(q2[1]), "+v"(q3[0]), "+v"(q3[1]),
                 "+v"(ka0), "+v"(ka1) :: "memory");
    f32x16 s0 = MFMA(frag(ka0), frag(q0[0]), zero), s1 = MFMA(frag(ka0), frag(q1[0]), zero);
    s0 = MFMA(frag(ka1), frag(q0[1]), s0); s1 = MFMA(frag(ka1), frag(q1[1]), s1);
    f32x16 s2 = MFMA(frag(ka0), frag(q2[0]), zero), s3 = MFMA(frag(ka0), frag(q3[0]), zero);
    s2 = MFMA(frag(ka1), frag(q2[1]), s2); s3 = MFMA(frag(ka1), frag(q3[1]), s3);
#pragma unroll
    for (int e_ = 0; e_ < 16; ++e_) { const int e = e_; m[0] = fmaxf(m[0], s0[e]); m[1] = fmaxf(m[1], s1[e]); m[2] = fmaxf(m[2], s2[e]); m[3] = fmaxf(m[3], s3[e]); }
  }
#pragma unroll
  for (int b = 0; b < 4; ++b) {
    m[b] = xhalf_max(m[b]);
    const float nm = -m[b];
    const unsigned u0 = __float_as_uint(nm) & 0xffff0000u;
    const float r1 = nm - __uint_as_float(u0);
    const unsigned u1 = __float_as_uint(r1) & 0xffff0000u;
    const float r2 = r1 - __uint_as_float(u1);
    u32x4& qq = b == 0 ? q0[1] : (b == 1 ? q1[1] : (b == 2 ? q2[1] : q3[1]));
    qq[2] = h ? ((u0 >> 16) | u1) : qq[2];
    qq[3] = h ? (__float_as_uint(r2) >> 16) : qq[3];
  }
  STAMP(1, 1);
  f32x16 sa = MFMA(frag(ka0), frag(q0[0]), zero), sb;
  sa = MFMA(frag(ka1), frag(q0[1]), sa);
  SBAR();
  ld16<3072>(vb1, rs, vo, sv + rot); ld16<0>(kc0, rs, vo, sk + rot1); ld16<1024>(kc1, rs, vo, sk + rot1); ld16<0>(vc0, rs, vo, sv + rot1);
  ld16<1024>(vc1, rs, vo, sv + rot1); ld16<2048>(kd0, rs, vo, sk + rot1); ld16<3072>(kd1, rs, vo, sk + rot1); ld16<2048>(vd0, rs, vo, sv + rot1);
  unsigned oc = rot;
#define ATT_TILE(K0, K1, V0, V1, KN0, KN1, VP1, VP1_OFF, VP1_S, OFF, KS, VS)                                              \
    ATT_UNIT(sa, sb, K0, K1, q1, o[3], VP1, o[0], V0, (void)0, (void)0,                                                   \
             asm volatile("s_waitcnt vmcnt(11)" : "+v"(V0), "+v"(V1) :: "memory"), ld16<VP1_OFF>(VP1, rs, vo, VP1_S));    \
    ATT_UNIT(sb, sa, K0, K1, q2, o[0], V1, o[1], V0, (void)0, (void)0, (void)0, (void)0);                                 \
    ATT_UNIT(sa, sb, K0, K1, q3, o[1], V1, o[2], V0, ld16<OFF>(K0, rs, vo, KS), (void)0, ld16<OFF + 1024>(K1, rs, vo, KS), \
             asm volatile("s_waitcnt vmcnt(12)" : "+v"(KN0), "+v"(KN1) :: "memory"));                                     \
    ATT_UNIT(sb, sa, KN0, KN1, q0, o[2], V1, o[3], V0, (void)0, (void)0, (void)0, ld16<OFF>(V0, rs, vo, VS))
#pragma unroll 1
  for (int i = 0;; ++i) {
    const unsigned c1 = (oc + 4096u) & 32767u, n0 = (oc + 8192u) & 32767u, n1 = (oc + 12288u) & 32767u;
    const unsigned k0 = i < 3 ? sk + n0 : OOB, k1 = i < 3 ? sk + n1 : OOB, v0 = i < 3 ? sv + n0 : OOB, v1 = i < 3 ? sv + n1 : OOB;
    ATT_TILE(ka0, ka1, va0, va1, kb0, kb1, vd1, 3072, sv + c1, 0, k0, v0);
    ATT_TILE(kb0, kb1, vb0, vb1, kc0, kc1, va1, 1024, v0, 2048, k0, v0);
    ATT_TILE(kc0, kc1, vc0, vc1, kd0, kd1, vb1, 3072, v0, 0, k1, v1);
    if (i == 3) break;
    ATT_TILE(kd0, kd1, vd0, vd1, ka0, ka1, vc1, 1024, v1, 2048, k1, v1);
    oc = n0;
  }
  lptr xa[3];
  unsigned l16 = vo;
  PIN(l16);
#pragma unroll
  for (int b = 0; b < 3; ++b) xa[b] = c.lds + L_XA + (((w + 1 + b) & 3) * 3 + b) * 4096 + l16;
#define XA_OUT(B, G) do { *(LAS f32x4*)(xa[B] + (G) * 1024) = (f32x4){o[B][4 * (G)], o[B][4 * (G) + 1], o[B][4 * (G) + 2], o[B][4 * (G) + 3]}; SBAR(); } while (0)
  ATT_UNIT(sa, sb, kd0, kd1, q1, o[3], vc1, o[0], vd0, (void)0, (void)0,
           asm volatile("s_waitcnt vmcnt(0)" : "+v"(ka0), "+v"(ka1), "+v"(va0), "+v"(va1), "+v"(kb0), "+v"(kb1), "+v"(vb0), "+v"(vb1),
                        "+v"(kc0), "+v"(kc1), "+v"(vc0), "+v"(vd0), "+v"(vd1) :: "memory"), (void)0);
  ATT_UNIT(sb, sa, kd0, kd1, q2, o[0], vd1, o[1], vd0, (void)0, (void)0, (void)0, (void)0);
  ATT_UNIT(sa, sb, kd0, kd1, q3, o[1], vd1, o[2], vd0, XA_OUT(0, 0), XA_OUT(0, 1), XA_OUT(0, 2), XA_OUT(0, 3));
  ATT_UNIT(sb, sa, kd0, kd1, q0, o[2], vd1, o[3], vd0, XA_OUT(1, 0), XA_OUT(1, 1), XA_OUT(1, 2), XA_OUT(1, 3));
  o[3] = SMF(vd1, e, o[3]);
  SBAR();
  XA_OUT(2, 0); XA_OUT(2, 1); XA_OUT(2, 2); XA_OUT(2, 3);
}

__device__ __forceinline__ void p2_attn(Ctx& c) {
  const int lane = c.lane, w = c.w, r = c.r, h = c.h;
  lptr lds = c.lds;
  const int hd = (blockIdx.x & 7) * 2 + ((blockIdx.x >> 3) & 1), rb = blockIdx.x >> 4;
  {
    const int u = lane < 16 ? 16 + hd : (lane < 32 ? 32 + hd : hd);
    acquire(c, flag1_off((u / 3) * 16 + (lane < 32 ? (lane & 15) : rb)), EV_GO2);
  }
  STAMP(1, 0);
  f32x16 o[4];
  float m[4];
  __builtin_amdgcn_s_setprio(3);
  attn_pass<false>(c, o, m);
  bool ok = true;
#pragma unroll
  for (int b = 0; b < 4; ++b) ok &= o[b][12] >= 0x1p-30f && o[b][12] <= 0x1p64f;
  if (!__all(ok || h == 0)) attn_pass<true>(c, o, m);
  STAMP(1, 2);
#pragma unroll
  for (int b = 0; b < 4; ++b)
    *(LAS f32x2*)(lds + (h ? L_ML + ((w + 1 + b) & 3) * 1024 + r * 32 + b * 8 : L_CS + r * 8)) = (f32x2){m[b], o[b][12]};
  ev_set(c, EV_XA, w);
  pin16(o[3]);
  ev_wait<1>(c, EV_MAP);
  c.map = __builtin_amdgcn_readfirstlane(*(volatile LAS unsigned*)(lds + L_CNT + EV_MAP * 16));
  f32x4 a0, a1, v[3][4];
  {
    const unsigned fa = c.lbase + L_CNT + EV_XA * 16, ma = c.lbase + L_ML + w * 1024 + r * 32, xa = c.lbase + L_XA + w * 12288 + lane * 16;
#pragma unroll 1
    for (int spins = 0; spins < (1 << 24); ++spins) {
      u32x4 f;
      asm volatile("ds_read_b128 %0, %15\n\tds_read_b128 %1, %16\n\tds_read_b128 %2, %16 offset:16\n\t"
                   "ds_read_b128 %3, %17\n\tds_read_b128 %4, %17 offset:1024\n\tds_read_b128 %5, %17 offset:2048\n\tds_read_b128 %6, %17 offset:3072\n\t"
                   "ds_read_b128 %7, %17 offset:4096\n\tds_read_b128 %8, %17 offset:5120\n\tds_read_b128 %9, %17 offset:6144\n\tds_read_b128 %10, %17 offset:7168\n\t"
                   "ds_read_b128 %11, %17 offset:8192\n\tds_read_b128 %12, %17 offset:9216\n\tds_read_b128 %13, %17 offset:10240\n\tds_read_b128 %14, %17 offset:11264\n\t"
                   "s_waitcnt lgkmcnt(0)"
                   : "=&v"(f), "=&v"(a0), "=&v"(a1), "=&v"(v[0][0]), "=&v"(v[0][1]), "=&v"(v[0][2]), "=&v"(v[0][3]), "=&v"(v[1][0]), "=&v"(v[1][1]),
                     "=&v"(v[1][2]), "=&v"(v[1][3]), "=&v"(v[2][0]), "=&v"(v[2][1]), "=&v"(v[2][2]), "=&v"(v[2][3])
                   : "v"(fa), "v"(ma), "v"(xa) : "memory");
      if (__builtin_amdgcn_readfirstlane(f[0] & f[1] & f[2] & f[3])) break;
      ev_wait<4>(c, EV_XA);
    }
  }
  STAMP(1, 4);
  const float M = fmaxf(fmaxf(a0[0], a0[2]), fmaxf(a1[0], a1[2]));
  float wk[4] = {EX(a0[0] - M), EX(a0[2] - M), EX(a1[0] - M), EX(a1[2] - M)};
  const float inv = __builtin_amdgcn_rcpf((a0[1] * wk[0] + a0[3] * wk[1]) + (a1[1] * wk[2] + a1[3] * wk[3]));
#pragma unroll
  for (int k = 0; k < 4; ++k) wk[k] *= inv;
  u32x4 res[2];
#pragma unroll
  for (int g = 0; g < 4; ++g) {
    const f32x4 own = {o[3][4 * g], o[3][4 * g + 1], o[3][4 * g + 2], o[3][4 * g + 3]};
    const f32x4 f = (own * wk[3] + v[0][g] * wk[0]) + (v[1][g] * wk[1] + v[2][g] * wk[2]);
    res[g >> 1][(g & 1) * 2] = pk2(f[0], f[1]); res[g >> 1][(g & 1) * 2 + 1] = pk2(f[2], f[3]);
  }
  if (h) { res[1][2] = 0; res[1][3] = 0; }
#pragma unroll
  for (int s = 0; s < 2; ++s) st16s(s, res[s], c.rws, lane * 16, base_b(c, rb >> 1) + spread(POS_O + (unsigned)(((rb & 1) * 4 + w) * 16 + hd) * 2048u));
  STAMP(1, 5);
}
__device__ __forceinline__ void p2_publish(const Ctx& c) {
  publish<4>(c, EV_PUB2, flag_off(c, 1, (blockIdx.x >> 4) * 16 + (blockIdx.x & 7) * 2 + ((blockIdx.x >> 3) & 1), blockIdx.x >> 5));
}

__device__ __forceinline__ void stats_issue(const Ctx& c, int t, f32x2 (&v)[7]) {
  const unsigned vo = c.h * 1792 + c.r * 8, so = base_a(c, blockIdx.x & 7) + spread(POS_ST + (t & 7) * 4096u);
#pragma unroll
  for (int i = 0; i < 7; ++i)
    asm volatile("buffer_load_dwordx2 %0, %1, %2, %3 offen offset:%4 sc1" : "=v"(v[i]) : "v"(vo), "s"(c.rws), "s"(so), "i"(i * 256) : "memory");
}
#define STATS_WAIT(N, v) asm volatile("s_waitcnt vmcnt(" #N ")" : "+v"(v[0]), "+v"(v[1]), "+v"(v[2]), "+v"(v[3]), "+v"(v[4]), "+v"(v[5]), "+v"(v[6]) :: "memory")
__device__ __forceinline__ void stats_pin(f32x2 (&v)[7]) { asm volatile("" : "+v"(v[0]), "+v"(v[1]), "+v"(v[2]), "+v"(v[3]), "+v"(v[4]), "+v"(v[5]), "+v"(v[6])); }
__device__ __forceinline__ void stats_finish(const f32x2 (&v)[7], float& mu, float& rstd) {
  const f32x2 t = ((v[0] + v[1]) + (v[2] + v[3])) + ((v[4] + v[5]) + v[6]);
  mu = xhalf_sum(t[0]) * (1.f / 448.f);
  rstd = __builtin_amdgcn_rsqf(fmaxf(xhalf_sum(t[1]) * (1.f / 448.f) - mu * mu, 0.f) + EPS);
}
__device__ __forceinline__ unsigned relu2(float a, float b) {
  return __builtin_bit_cast(unsigned, __builtin_elementwise_max(__builtin_bit_cast(s16x2, pk2(a, b)), (s16x2){0, 0}));
}
constexpr int w1_slot(int st) { return st; }
constexpr int w2_pos(int k) { return k < 35 ? k : k - 35; }

__device__ __forceinline__ void pair_reduce(const Ctx& c, f32x16 (&acc)[2], int lbuf, int ev) {
  lptr dst = c.lds + lbuf + c.w * 4096 + c.lane * 16;
#pragma unroll
  for (int g = 0; g < 4; ++g) *(LAS f32x4*)(dst + g * 1024) = (f32x4){acc[1][4 * g], acc[1][4 * g + 1], acc[1][4 * g + 2], acc[1][4 * g + 3]};
  ev_set(c, ev, c.w);
  PIN(acc[0]);
  const unsigned fa = c.lbase + L_CNT + ev * 16 + (c.w ^ 2) * 4, sa = c.lbase + lbuf + (c.w ^ 2) * 4096 + c.lane * 16;
  f32x4 v0, v1, v2, v3;
#pragma unroll 1
  for (int spins = 0; spins < (1 << 24); ++spins) {
    unsigned f;
    asm volatile("ds_read_b32 %0, %5\n\tds_read_b128 %1, %6\n\tds_read_b128 %2, %6 offset:1024\n\tds_read_b128 %3, %6 offset:2048\n\t"
                 "ds_read_b128 %4, %6 offset:3072\n\ts_waitcnt lgkmcnt(0)"
                 : "=&v"(f), "=&v"(v0), "=&v"(v1), "=&v"(v2), "=&v"(v3) : "v"(fa), "v"(sa) : "memory");
    if (__builtin_amdgcn_readfirstlane(f)) break;
  }
#pragma unroll
  for (int e = 0; e < 4; ++e) { acc[0][e] += v0[e]; acc[0][4 + e] += v1[e]; acc[0][8 + e] += v2[e]; acc[0][12 + e] += v3[e]; }
}

__device__ __forceinline__ void p345_compute(const Ctx& c, const char* x, char* out, int rb3, int cb3, int rb4, int cb4) {
  const int lane = c.lane, w = c.w, r = c.r, h = c.h;
  lptr lds = c.lds;
  const rsrc_t rs = c.rws;
  const int wr = w & 1, kh = w >> 1, q = (lane & 15) >> 2;
  const int t3 = rb3 * 2 + wr, sub = cb3 * 2 + kh;
  const unsigned trl = ((lane >> 4) & 1) * 32 + (lane & 3) * 8, va = lane * 16;
  const unsigned grp = blockIdx.x & 7, ba = base_a(c, grp), bb_ = base_b(c, grp);
  u32x4 hres[2];
  {
    u32x4 bw[16][2];
    u32x2 xr[4];
    ev_wait<4>(c, EV_WO);
    const unsigned b0 = L_WO + (kh * 224 + 4 * h + q) * 128 + trl;
    unsigned bb[2] = {b0 + ((kh ^ (q >> 1)) * 64), b0 + ((kh ^ 1 ^ (q >> 1)) * 64)};
    PIN(bb[0]); PIN(bb[1]);
#pragma unroll
    for (int ks = 0; ks < 16; ++ks) {
      if (ks == 4) {
        SBAR(); p2_publish(c); STAMP(1, 6);
        const rsrc_t rx = mkrs(x, S_ * D_ * 2);
        const unsigned vx = (unsigned)(t3 * 32 + r) * 896u + h * 8 + sub * 64u;
#pragma unroll
        for (int g = 0; g < 4; ++g) asm volatile("buffer_load_dwordx2 %0, %1, %2, 0 offen offset:%3" : "=v"(xr[g]) : "v"(vx), "s"(rx), "i"(g * 16) : "memory");
      }
#pragma unroll
      for (int i = 0; i < 2; ++i) bw[ks][i] = ldstr2(lds + bb[i] + ((ks >> 1) * 28 + (ks & 1) * 16) * 128, 1024);
    }
    ev_set(c, EV_BPRE, w);
    ev_wait<4>(c, EV_BPRE);
    acquire(c, flag_off(c, 1, (rb3 >> 1) * 16, grp) + (lane & 15) * 256, EV_GO3);
    STAMP(2, 0);
    const unsigned sa = bb_ + 2u * (POS_O + (unsigned)((t3 & 7) * 32 + kh * 16) * 1024u);
    auto so = [&](int g) { return sa + (g & 1) * 4096u + (g >> 1) * 16384u; };
    u32x4 a[16];
    static_for<0, 10>([&](auto kc) { constexpr int k = decltype(kc)::value; ld16<(k & 3) * 1024>(a[k], rs, va, so(k >> 2)); });
    asm volatile("s_waitcnt vmcnt(10)" : "+v"(xr[0]), "+v"(xr[1]), "+v"(xr[2]), "+v"(xr[3]) :: "memory");
    f32x16 acc[2];
#pragma unroll
    for (int g = 0; g < 4; ++g)
#pragma unroll
      for (int i = 0; i < 2; ++i) { acc[0][4 * g + 2 * i] = bflo(xr[g][i]); acc[0][4 * g + 2 * i + 1] = bfhi(xr[g][i]); }
    acc[1] = f32x16{};
    pin16(acc[0]); pin16(acc[1]);
    static_for<0, 16>([&](auto kc) {
      constexpr int k = decltype(kc)::value;
      waitv<(k <= 6 ? 9 : 15 - k)>(a[k]);
      acc[1] = MFMA(frag(bw[k][1]), frag(a[k]), acc[1]);
      SBAR();
      if constexpr (k + 10 < 16) ld16<((k + 10) & 3) * 1024>(a[k + 10], rs, va, so((k + 10) >> 2));
      SBAR();
      acc[0] = MFMA(frag(bw[k][0]), frag(a[k]), acc[0]);
      SBAR();
    });
    STAMP(2, 7);
    pair_reduce(c, acc, L_XB, EV_RED3);
    STAMP(2, 8);
#pragma unroll
    for (int i = 0; i < 8; ++i) hres[i >> 2][i & 3] = pk2(acc[0][2 * i], acc[0][2 * i + 1]);
#pragma unroll
    for (int sx = 0; sx < 2; ++sx) st16s(sx, hres[sx], rs, va, bb_ + spread(POS_H + (unsigned)((t3 & 7) * 32 + sub * 2) * 1024u));
    f32x4 s4 = {}, q4 = {};
#pragma unroll
    for (int g = 0; g < 4; ++g) {
      const f32x4 v = {acc[0][4 * g], acc[0][4 * g + 1], acc[0][4 * g + 2], acc[0][4 * g + 3]};
      s4 += v; q4 += v * v;
    }
    const float s = xhalf_sum((s4[0] + s4[1]) + (s4[2] + s4[3])), sq = xhalf_sum((q4[0] + q4[1]) + (q4[2] + q4[3]));
    if (h == 0) __builtin_amdgcn_raw_buffer_store_b64((u32x2){__float_as_uint(s), __float_as_uint(sq)}, rs, r * 8, ba + spread(POS_ST + (t3 & 7) * 4096u) + sub * 256u, 16);
    STAMP(2, 9);
  }
  {
    const int t4 = rb4 * 4 + w;
    unsigned bo[4][3];
#pragma unroll
    for (int i = 0; i < 4; ++i) {
      bo[i][0] = (4 * h + q) * 256 + ((i ^ q) * 64) + trl; bo[i][1] = bo[i][0] + 65536; bo[i][2] = bo[i][0] + 131072;
      PIN(bo[i][0]); PIN(bo[i][1]); PIN(bo[i][2]);
    }
    publish<4>(c, EV_PUB3, flag_off(c, 2, rb3 * 8 + cb3, grp));
    STAMP(2, 10);
    ev_wait<4>(c, EV_CS);
    u32x2 cf[4];
#pragma unroll
    for (int i = 0; i < 4; ++i) { const u32x4 t = colfrag(*(LAS float*)(lds + L_CS + i * 128 + r * 4), h); cf[i] = (u32x2){t[0], t[1]}; PIN(cf[i]); }
    u32x4 a[28], bq[4][2];
    auto bread = [&](auto kc) {
      constexpr int k = decltype(kc)::value, pass = k / 28, ks = k % 28, off = w1_slot(ks >> 2) * SLOT + (ks & 3) * 4096;
      bq[k & 3][0] = ldstr2(lds + bo[2 * pass][off >> 16] + (off & 65535), 2048);
      bq[k & 3][1] = ldstr2(lds + bo[2 * pass + 1][off >> 16] + (off & 65535), 2048);
    };
    bread(IC<0>{}); bread(IC<1>{}); bread(IC<2>{});
    SBAR();
    const int rot = p4_rot(cb4);
    unsigned sa4[7];
#pragma unroll
    for (int g = 0; g < 7; ++g) { sa4[g] = bb_ + 2u * (POS_H + (t4 & 7) * 32768u) + spread((g + rot >= 7 ? g + rot - 7 : g + rot) * 4096u); asm volatile("" : "+s"(sa4[g])); }
    const unsigned sg = ba + 2u * (POS_G + (unsigned)((t4 & 7) * 112 + cb4 * 8) * 1024u);
    { const int l = lane < 14 ? lane : 0; acquire(c, flag_off(c, 2, rb4 * 16, grp) + (l >= 7 ? l + 1 : l) * 256, EV_GO4); }
    STAMP(3, 0);
    auto sa = [&](int g) { return sa4[g]; };
    f32x2 sv[7];
    static_for<0, 12>([&](auto kc) { constexpr int k = decltype(kc)::value; ld16<(k & 3) * 1024>(a[k], rs, va, sa(k >> 2)); });
    f32x16 acc[4] = {};
    u32x4 og, rf;
    static_for<0, 56>([&](auto kc) {
      constexpr int k = decltype(kc)::value, pass = k / 28, ks = k % 28;
      if constexpr (k < 28) waitv<((k + 11 < 27 ? k + 11 : 27) - k + (k >= 9 && k <= 20 ? 7 : 0))>(a[k]);
      if constexpr (k == 28) {
#pragma unroll
        for (int i = 0; i < 4; ++i) acc[i] = MFMA(frag((u32x4){cf[i][0], cf[i][1], 0u, 0u}), frag(rf), acc[i]);
        SBAR();
      }
      acc[2 * pass] = MFMA(frag(bq[k & 3][0]), frag(a[ks]), acc[2 * pass]);
      SBAR();
      if constexpr (k + 12 < 28) ld16<((k + 12) & 3) * 1024>(a[k + 12], rs, va, sa((k + 12) >> 2));
      if constexpr (k == 8) stats_issue(c, t4, sv);
      if constexpr (k + 3 < 56) bread(IC<k + 3>{});
      SBAR();
      acc[2 * pass + 1] = MFMA(frag(bq[k & 3][1]), frag(a[ks]), acc[2 * pass + 1]);
      SBAR();
      if constexpr (k == 21) {
        stats_pin(sv);
        float t = sv[0][0];
#pragma unroll
        for (int i = 1; i < 7; ++i) { t += sv[i][0]; pinf(t); }
        rf = rowfrag(xhalf_sum(t) * (-1.f / 448.f));
        pin4(rf);
        SBAR();
      }
      if constexpr (k == 24) { ev_set(c, EV_A4, w); SBAR(); }
      if constexpr (k >= 30 && k < 46) {
        constexpr int e = k - 30;
        og[e & 3] = relu2(acc[e >> 3][(e & 7) * 2], acc[e >> 3][(e & 7) * 2 + 1]);
        PIN(og);
        if constexpr ((e & 3) == 3) st16<(e >> 2) * 1024>(og, rs, va, sg);
        SBAR();
      }
    });
    ev_set(c, EV_X4, w);
    STAMP(3, 8);
    static_for<0, 4>([&](auto fc) {
      constexpr int f = decltype(fc)::value;
      u32x4 o;
#pragma unroll
      for (int e = 0; e < 4; ++e) o[e] = relu2(acc[2 + (f >> 1)][8 * (f & 1) + 2 * e], acc[2 + (f >> 1)][8 * (f & 1) + 2 * e + 1]);
      st16<f * 1024>(o, rs, va, sg + 4096u);
    });
    STAMP(3, 9);
    publish<4>(c, EV_PUB4, flag_off(c, 3, rb4 * 16 + cb4, grp));
    STAMP(3, 10);
  }
  {
    unsigned bo[2][3];
#pragma unroll
    for (int i = 0; i < 2; ++i) {
      bo[i][0] = kh * 2048 + (4 * h + q) * 128 + ((kh ^ i ^ (q >> 1)) * 64) + trl; bo[i][1] = bo[i][0] + 65536; bo[i][2] = bo[i][0] + 131072;
      PIN(bo[i][0]); PIN(bo[i][1]); PIN(bo[i][2]);
    }
    const int rot = p5_rot(cb3);
    const unsigned s5a = ba + 2u * (POS_G + (unsigned)((t3 & 7) * 112 + kh * 56 + rot * 4) * 1024u), s5b = rot ? s5a - 2u * 57344u : s5a;
    acquire(c, flag_off(c, 3, (rb3 >> 1) * 16, grp) + (lane < 14 ? lane : 0) * 256, EV_GO5);
    STAMP(4, 0);
    auto sa = [&](int g) { return (g >= 8 ? s5b : s5a) + spread(g * 4096u); };
    u32x4 a[W5], bq[4][2];
    f32x2 sv[7];
    static_for<0, W5>([&](auto kc) { constexpr int k = decltype(kc)::value; ld16<(k & 3) * 1024>(a[k], rs, va, sa(k >> 2)); });
    auto bread = [&](auto kc) {
      constexpr int k = decltype(kc)::value, off = w2_pos(k) * 4096;
      bq[k & 3][0] = ldstr2(lds + bo[0][off >> 16] + (off & 65535), 1024);
      bq[k & 3][1] = ldstr2(lds + bo[1][off >> 16] + (off & 65535), 1024);
    };
    ev_wait_half(c, EV_W2A);
    bread(IC<0>{}); bread(IC<1>{}); bread(IC<2>{});
    f32x16 acc[2] = {};
    static_for<0, 56>([&](auto kc) {
      constexpr int k = decltype(kc)::value;
      waitv<(k <= 56 - W5 ? W5 - 1 : 55 - k + 7)>(a[k % W5]);
      acc[1] = MFMA(frag(bq[k & 3][1]), frag(a[k % W5]), acc[1]);
      SBAR();
      if constexpr (k == 4 || k == 11 || k == 18) ev_set(c, EV_SEG0 + (k - 4) / 7, w);
      if constexpr (k == 32) { pin16(acc[0]); pin16(acc[1]); ev_wait_half(c, EV_W2B); }
      if constexpr (k + 3 < 56) bread(IC<k + 3>{});
      SBAR();
      acc[0] = MFMA(frag(bq[k & 3][0]), frag(a[k % W5]), acc[0]);
      SBAR();
      if constexpr (k + W5 < 56) ld16<((k + W5) & 3) * 1024>(a[k % W5], rs, va, sa((k + W5) >> 2));
      if constexpr (k == 56 - W5) stats_issue(c, t3, sv);
      SBAR();
    });
    STAMP(4, 15);
    float mu, rstd;
    STATS_WAIT(0, sv);
    stats_finish(sv, mu, rstd);
    pair_reduce(c, acc, L_XB, EV_RED5);
    STAMP(4, 16);
#pragma unroll
    for (int sx = 0; sx < 2; ++sx) {
      u32x4 o;
#pragma unroll
      for (int i = 0; i < 4; ++i) o[i] = pk2(acc[0][8 * sx + 2 * i] * rstd + bflo(hres[sx][i]), acc[0][8 * sx + 2 * i + 1] * rstd + bfhi(hres[sx][i]));
      auto x0 = __builtin_amdgcn_permlane32_swap(o[0], o[2], false, false);
      auto x1 = __builtin_amdgcn_permlane32_swap(o[1], o[3], false, false);
      *(u32x4*)(out + (unsigned)(t3 * 32 + r) * 896u + (sub * 32 + 16 * sx + 8 * h) * 2) = (u32x4){x0[0], x1[0], x0[1], x1[1]};
    }
    STAMP(4, 18);
  }
}

__device__ __forceinline__ void probe(const Ctx& c, int id, unsigned off) {
  unsigned best[2] = {~0u, ~0u};
#pragma unroll 1
  for (int i = 0; i < 8; ++i) {
    unsigned long long t0, t1;
    const unsigned z = 0;
    asm volatile("s_memtime %0\n\ts_waitcnt lgkmcnt(0)\n\tbuffer_store_dword %2, off, %3, %4 sc1\n\ts_waitcnt vmcnt(0)\n\ts_memtime %1\n\ts_waitcnt lgkmcnt(0)"
                 : "=&s"(t0), "=&s"(t1) : "v"(z), "s"(c.rws), "s"(off + (i & 1) * 8192u) : "memory");
    const unsigned d = (unsigned)(t1 - t0);
    if (i & 1) best[1] = min(best[1], d); else best[0] = min(best[0], d);
  }
  if (c.lane == 0) __builtin_amdgcn_raw_buffer_store_b64((u32x2){best[1] < best[0] ? 1u : 0u, c.tag}, c.rws, id * 8, OFF_PR, 16);
}
__device__ __forceinline__ void fetch_map(const Ctx& c) {
  const unsigned vo = (c.lane & 15) * 8;
  u32x2 g;
#pragma unroll 1
  for (int spins = 0; spins < (1 << 17); ++spins) {
    asm volatile("buffer_load_dwordx2 %0, %1, %2, %3 offen sc1\n\ts_waitcnt vmcnt(0)" : "=v"(g) : "v"(vo), "s"(c.rws), "s"(OFF_PR) : "memory");
    if (__all(g[1] == c.tag)) break;
    __builtin_amdgcn_s_sleep(8);
  }
  ev_set(c, EV_MAP, 0, ((unsigned)__ballot(g[0] & 1) & 0xFFFFu) | 0x80000000u);
}

__device__ __forceinline__ void helper(const Ctx& c, const char* wo, const char* w1, const char* w2, int cb3, int cb4) {
  const int lane = c.lane, w = c.w;
  const rsrc_t rwo = mkrs(wo, D_ * D_ * 2), rw1 = mkrs(w1, D_ * DFF * 2), rw2 = mkrs(w2, D_ * DFF * 2);
  const unsigned vw1 = (unsigned)(lane >> 4) * 3584u + (unsigned)((lane & 15) ^ ((lane >> 4) << 2)) * 16u;
  const unsigned vw2 = (unsigned)(lane >> 3) * 896u + (unsigned)((lane & 7) ^ (((lane >> 4) & 1) << 2)) * 16u;
  const int rot5 = p5_rot(cb3) * 4;
  const unsigned sw0 = (unsigned)((w >> 1) * 896 + (w & 1) * 8 + rot5 * 16) * 896u + cb3 * 128u;
  auto sw = [&](int k) { return sw0 + k * 14336u - (k + rot5 >= 56 ? 56u * 14336u : 0u); };
  u32x4 s1[16], st[49];
  auto ldw = [&](auto ic) { constexpr int i = decltype(ic)::value, k = i < 28 ? i : i + 7; ld16p(st[i], rw2, vw2, sw(k)); };
  ev_wait<1, 4>(c, EV_GO2);
  __builtin_amdgcn_s_sleep(32);
#pragma unroll 1
  for (int p = w; p < 57; p += 4) { dma16(rwo, vw2, p * 7168u + cb3 * 128u, c.lbase + L_WO + p * 1024); __builtin_amdgcn_s_sleep(PACE); }
#pragma unroll 1
  for (int p = w; p < 48; p += 4) { dma16(rw1, vw1, w1_piece(p, cb4), c.lbase + p * 1024); __builtin_amdgcn_s_sleep(PACE); }
  static_for<0, 16>([&](auto ic) { constexpr int i = decltype(ic)::value; ld16p(s1[i], rw1, vw1, w1_piece(48 + i * 4 + w, cb4)); __builtin_amdgcn_s_sleep(PACE); });
  static_for<0, 42>([&](auto ic) {
    ldw(ic);
    __builtin_amdgcn_s_sleep(PACE);
    if constexpr (decltype(ic)::value == 15) { waitv<16>(s1[15]); ev_set(c, EV_WO, w); }
  });
  STAMP(1, 16);
  if (w == 0) fetch_map(c);
  STAMP(1, 17);
  unsigned wb[3] = {(unsigned)(w * 1024 + lane * 16), 0u, 0u};
  wb[1] = wb[0] + 65536; wb[2] = wb[0] + 131072;
  PIN(wb[0]); PIN(wb[1]); PIN(wb[2]);
  ev_wait<4, 1>(c, EV_BPRE);
  static_for<0, 16>([&](auto ic) {
    constexpr int i = decltype(ic)::value, off = 3 * SLOT + i * 4096;
    waitv<42>(s1[i]);
    *(LAS u32x4*)(c.lds + wb[off >> 16] + (off & 65535)) = s1[i];
  });
  ev_set(c, EV_W1, w);
  ev_wait<4>(c, EV_W1);
  {
    const int q = (lane & 15) >> 2;
    lptr bp = c.lds + (4 * c.h + q) * 256 + ((w ^ q) * 64) + ((lane >> 4) & 1) * 32 + (lane & 3) * 8;
    float cs[4] = {0.f, 0.f, 0.f, 0.f};
#pragma unroll 1
    for (int sl = 0; sl < 2; ++sl) {
      u32x2 b[28];
#pragma unroll
      for (int k = 0; k < 28; ++k) b[k] = ldstr(bp + sl * 57344 + k * 2048);
      SBAR();
#pragma unroll
      for (int k = 0; k < 28; ++k) { cs[(k & 1) * 2] = dot2(b[k][0], ONES2, cs[(k & 1) * 2]); cs[(k & 1) * 2 + 1] = dot2(b[k][1], ONES2, cs[(k & 1) * 2 + 1]); }
    }
    *(LAS float*)(c.lds + L_CS + w * 128 + c.r * 4) = xhalf_sum((cs[0] + cs[1]) + (cs[2] + cs[3]));
    ev_set(c, EV_CS, w);
  }
  ev_wait<4, 2>(c, EV_A4);
#pragma unroll 1
  for (int k = 28; k < 35; ++k) dma16(rw2, vw2, sw(k), c.lbase + k * 4096 + w * 1024);
  static_for<42, 49>(ldw);
  auto stw = [&](auto ic) {
    constexpr int i = decltype(ic)::value, k = i < 28 ? i : i + 7, off = w2_pos(k) * 4096;
    waitv<0>(st[i]);
    *(LAS u32x4*)(c.lds + wb[off >> 16] + (off & 65535)) = st[i];
  };
  ev_wait<4, 2>(c, EV_X4);
  static_for<0, 28>(stw);
  ev_set(c, EV_W2A, w);
  ev_wait_half<2>(c, EV_SEG0);
  static_for<28, 35>(stw);
  ev_wait_half<2>(c, EV_SEG1);
  static_for<35, 42>(stw);
  ev_wait_half<2>(c, EV_SEG2);
  static_for<42, 49>(stw);
  ev_set(c, EV_W2B, w);
}

__global__ __launch_bounds__(512, 2) void block_fwd(const char* __restrict__ x, const char* __restrict__ wqkv, const char* __restrict__ wo,
                                                    const char* __restrict__ w1, const char* __restrict__ w2,
                                                    char* __restrict__ ws, char* __restrict__ out) {
  extern __shared__ __attribute__((aligned(16))) char smem[];
  Ctx c;
  c.lds = (lptr)smem; c.lbase = (unsigned)(uintptr_t)smem;
  c.lane = threadIdx.x & 63; c.w8 = __builtin_amdgcn_readfirstlane(threadIdx.x >> 6); c.w = c.w8 & 3;
  c.r = c.lane & 31; c.h = c.lane >> 5;
  const int xcd = blockIdx.x & 7, j = blockIdx.x >> 3;
  {
    unsigned long long pc;
    asm volatile("s_getpc_b64 %0" : "=s"(pc));
    touch_lds(c, mkrs((const void*)(pc & ~255ull), CODE_BYTES), c.lane < 2 ? c.lane * 128u : OOB, (c.w8 * 32 + j) * 256u);
  }
  asm volatile("" :: "s"(x), "s"(wqkv), "s"(wo), "s"(w1), "s"(w2), "s"(ws), "s"(out));
  c.ws = ws;
  c.wb = (char*)(((uintptr_t)ws + 0x1FFFFFull) & ~0x1FFFFFull);
  c.map = 0;
  c.rws = mkrs(c.wb, WS_USED);
  const int rb1 = (xcd >> 1) * 4 + (j & 3), cb1 = (xcd & 1) * 8 + (j >> 2);
  const int rb3 = xcd * 4 + (j & 3), cb3 = j >> 2, rb4 = xcd * 2 + (j & 1), cb4 = j >> 1;
  c.tag = 0;
  if (c.w8 >= 4) {
    STAMP(0, 0);
    p1_load(c, x, wqkv, rb1, cb1);
  } else {
    p1_stats(c, x, wqkv, wo, w1, w2, rb1, cb1);
  }
  int role = c.w8;
  asm volatile("" : "+s"(role));
  if (role >= 4) {
    ev_wait<4, 1>(c, EV_P1D);
    c.tag = __builtin_amdgcn_readfirstlane(*(volatile LAS unsigned*)(c.lds + L_CNT + EV_TAG * 16));
    if (j < 28) helper(c, wo, w1, w2, cb3, cb4);
    else {
      ev_wait<1, 4>(c, EV_GO2);
      __builtin_amdgcn_s_sleep(32);
      if (c.w == 1 && j == 28) probe(c, xcd, OFF_A + (xcd << 21) + 0x1F0000u);
      if (c.w == 1 && j == 29) probe(c, 8 + xcd, OFF_B + (xcd << 20) + spread(POS_H + 28672u));
      if (c.w == 0) { __builtin_amdgcn_s_sleep(100); fetch_map(c); }
    }
    return;
  }
  if (cb1 < 10) p1_compute<3>(c, rb1, cb1);
  else if (cb1 == 10) p1_compute<2>(c, rb1, cb1);
  else p1_compute<0>(c, rb1, cb1);
  publish<4>(c, EV_PUB1, flag1_off(cb1 * 16 + rb1));
  ev_set(c, EV_TAG, 0, c.tag);
  ev_set(c, EV_P1D, c.w);
  STAMP(0, 10);
  p2_attn(c);
  if (j >= 28) {
    p2_publish(c);
    if (blockIdx.x == 255 && c.w8 == 0) {
#pragma unroll 1
      for (int i = 0; i < 4; ++i) poll(c, flag1_off(i * 64 + c.lane));
      if (c.lane == 0) __builtin_amdgcn_raw_buffer_store_b32(c.tag, c.rws, 0, OFF_EP, 16);
    }
    return;
  }
  p345_compute(c, x, out, rb3, cb3, rb4, cb4);
}

extern "C" void kernel_launch(void* const* d_in, const int* in_sizes, int n_in, void* d_out, int out_size,
                              void* d_ws, size_t ws_size, hipStream_t stream) {
  (void)hipFuncSetAttribute((const void*)block_fwd, hipFuncAttributeMaxDynamicSharedMemorySize, LDS_BYTES);
  block_fwd<<<256, 512, LDS_BYTES, stream>>>((const char*)d_in[0], (const char*)d_in[1], (const char*)d_in[2],
                                             (const char*)d_in[4], (const char*)d_in[5], (char*)d_ws, (char*)d_out);
}
```

```cpp
#include <hip/hip_runtime.h>
#include <hip/hip_bf16.h>
#include <stdint.h>

typedef __attribute__((ext_vector_type(8))) short bf16x8;
typedef __attribute__((ext_vector_type(4))) short s16x4;
typedef __attribute__((ext_vector_type(2))) short s16x2;
typedef __attribute__((ext_vector_type(2))) __bf16 bf16x2;
typedef __attribute__((ext_vector_type(16))) float f32x16;
typedef __attribute__((ext_vector_type(8))) __bf16 bf16x8n;
typedef __attribute__((ext_vector_type(16))) __bf16 bf16x16n;
typedef __attribute__((ext_vector_type(8))) float f32x8;
typedef __attribute__((ext_vector_type(4))) float f32x4;
typedef __attribute__((ext_vector_type(2))) float f32x2;
typedef __attribute__((ext_vector_type(4))) unsigned u32x4;
typedef __attribute__((ext_vector_type(2))) unsigned u32x2;
typedef __amdgpu_buffer_rsrc_t rsrc_t;
#define LAS __attribute__((address_space(3)))
typedef LAS char* lptr;

constexpr int S_ = 2048, D_ = 448, DFF = 1792;
constexpr unsigned OFF_F1 = 12u << 20;
constexpr unsigned OFF_EP = 13u << 20;
constexpr unsigned OFF_PR = OFF_EP + 256u;
constexpr unsigned OFF_LN = OFF_PR + 256u;
constexpr unsigned OFF_A = 16u << 20, OFF_B = 32u << 20, WS_USED = 40u << 20;
constexpr unsigned POS_G = 0, POS_ST = 8u * 112u * 1024u, POS_FL = POS_ST + 8u * 4096u;
constexpr unsigned POS_O = 0, POS_H = 8u * 32768u;
constexpr unsigned OOB = 0x7F000000u;
constexpr int SLOT = 16384, L_XA = 3 * SLOT, L_WO = 6 * SLOT, L_ML = L_WO + 57 * 1024, L_XB = 35 * 4096, LDS_BYTES = 163840;
constexpr int L_CS = L_ML + 4096, L_ROW = L_ML, L_CNT = LDS_BYTES - 1024, L_DUMMY = L_CNT + 512;
constexpr int P1_SLOT = 16384 + 12288;
enum { EV_PUB1, EV_PUB2, EV_PUB3, EV_PUB4, EV_GO2, EV_GO3, EV_GO4, EV_GO5, EV_XA, EV_WO, EV_BPRE, EV_W1, EV_CS,
       EV_P1D, EV_GAP, EV_TAG, EV_MAP, EV_RED3, EV_A4, EV_X4, EV_W2A, EV_SEG0, EV_SEG1, EV_SEG2, EV_W2B, EV_RED5, EV_COUNT };

constexpr unsigned CODE_BYTES = 43008;
#define STAG 8
#define ADELAY 16
#define HDELAY 4
#define NW2 38
#define PACE 1
#define W5 14
constexpr float QSCALE = 0.18898223650461362f * 1.4426950408889634f;
constexpr float EPS = 1e-5f;
constexpr unsigned ONES2 = 0x3F803F80u;

#define MFMA(a, b, c) __builtin_amdgcn_mfma_f32_32x32x16_bf16((a), (b), (c), 0, 0, 0)
#define WAITV_BAR(N) asm volatile("s_waitcnt vmcnt(" #N ")\n\ts_barrier" ::: "memory")
#define BAR() asm volatile("s_waitcnt lgkmcnt(0)\n\ts_barrier" ::: "memory")
#define SBAR() __builtin_amdgcn_sched_barrier(0)
#define PIN(x) asm volatile("" : "+v"(x))
#define EX(v) __builtin_amdgcn_exp2f(v)
#define STAMP(kid, i) do {} while (0)

__device__ __forceinline__ rsrc_t mkrs(const void* p, unsigned bytes) {
  return __builtin_amdgcn_make_buffer_rsrc((void*)p, (short)0, (int)bytes, 0x00020000);
}
__device__ __forceinline__ void dma16(rsrc_t r, unsigned voff, unsigned soff, unsigned ldsaddr) {
  asm volatile("s_mov_b32 m0, %0\n\ts_nop 0\n\tbuffer_load_dwordx4 %1, %2, %3 offen lds"
               :: "s"(ldsaddr), "v"(voff), "s"(r), "s"(soff) : "memory");
}
__device__ __forceinline__ void dma16x4_sc1(rsrc_t r, unsigned voff, unsigned soff, unsigned ldsaddr) {
  asm volatile("s_mov_b32 m0, %0\n\ts_nop 0\n\tbuffer_load_dwordx4 %1, %2, %3 offen sc1 lds\n\t"
               "buffer_load_dwordx4 %1, %2, %3 offen offset:1024 sc1 lds\n\t"
               "buffer_load_dwordx4 %1, %2, %3 offen offset:2048 sc1 lds\n\t"
               "buffer_load_dwordx4 %1, %2, %3 offen offset:3072 sc1 lds"
               :: "s"(ldsaddr), "v"(voff), "s"(r), "s"(soff) : "memory");
}
template <int IMM>
__device__ __forceinline__ void ld16(u32x4& d, rsrc_t r, unsigned voff, unsigned soff) {
  asm volatile("buffer_load_dwordx4 %0, %1, %2, %3 offen offset:%4 sc1" : "=v"(d) : "v"(voff), "s"(r), "s"(soff), "i"(IMM) : "memory");
}
__device__ __forceinline__ void ld16p(u32x4& d, rsrc_t r, unsigned voff, unsigned soff) {
  asm volatile("buffer_load_dwordx4 %0, %1, %2, %3 offen" : "=v"(d) : "v"(voff), "s"(r), "s"(soff) : "memory");
}
template <int N>
__device__ __forceinline__ void waitv(u32x4& x) { asm volatile("s_waitcnt vmcnt(%1)" : "+v"(x) : "i"(N) : "memory"); }
template <int IMM>
__device__ __forceinline__ void st16(u32x4 v, rsrc_t r, unsigned voff, unsigned soff) {
  asm volatile("buffer_store_dwordx4 %0, %1, %2, %3 offen offset:%4 sc1\n\ts_nop 1" :: "v"(v), "v"(voff), "s"(r), "s"(soff), "i"(IMM) : "memory");
}
__device__ __forceinline__ void st16s(int s, u32x4 v, rsrc_t r, unsigned voff, unsigned soff) {
  if (s) st16<1024>(v, r, voff, soff); else st16<0>(v, r, voff, soff);
}
__device__ __forceinline__ unsigned pk2(float a, float b) {
  f32x2 v = {a, b};
  return __builtin_bit_cast(unsigned, __builtin_convertvector(v, bf16x2));
}
__device__ __forceinline__ float bflo(unsigned u) { return __uint_as_float(u << 16); }
__device__ __forceinline__ float bfhi(unsigned u) { return __uint_as_float(u & 0xffff0000u); }
__device__ __forceinline__ float dot2(unsigned a, unsigned b, float c) {
  return __builtin_amdgcn_fdot2_f32_bf16(__builtin_bit_cast(bf16x2, a), __builtin_bit_cast(bf16x2, b), c, false);
}
__device__ __forceinline__ u32x4 lds128(lptr p) { return *(LAS u32x4*)p; }
__device__ __forceinline__ f32x4 ldsf4(lptr p) { return *(LAS f32x4*)p; }
__device__ __forceinline__ u32x2 ldstr(lptr p) {
  return __builtin_bit_cast(u32x2, __builtin_amdgcn_ds_read_tr16_b64_v4i16((LAS s16x4*)p));
}
__device__ __forceinline__ u32x4 ldstr2(lptr p, int d) {
  const u32x2 a = ldstr(p), b = ldstr(p + d);
  return (u32x4){a[0], a[1], b[0], b[1]};
}
__device__ __forceinline__ bf16x8 frag(u32x4 v) { return __builtin_bit_cast(bf16x8, v); }
__device__ __forceinline__ float xhalf_sum(float v) {
  auto r = __builtin_amdgcn_permlane32_swap(__float_as_uint(v), __float_as_uint(v), false, false);
  return __uint_as_float(r[0]) + __uint_as_float(r[1]);
}
__device__ __forceinline__ float xhalf_max(float v) {
  auto r = __builtin_amdgcn_permlane32_swap(__float_as_uint(v), __float_as_uint(v), false, false);
  return fmaxf(__uint_as_float(r[0]), __uint_as_float(r[1]));
}
__device__ __forceinline__ u32x4 colfrag(float cs, int h) {
  const unsigned a = pk2(cs, cs);
  const float lo = cs - bflo(a);
  const unsigned b = pk2(lo, lo);
  return (u32x4){h ? 0u : a, h ? 0u : b, 0u, 0u};
}
__device__ __forceinline__ u32x4 rowfrag(float nmu) {
  const unsigned a = pk2(nmu, nmu);
  const unsigned p = pk2(nmu, nmu - bflo(a));
  return (u32x4){p, p, 0u, 0u};
}

__device__ __forceinline__ void pin16(f32x16& x) { PIN(x); }
__device__ __forceinline__ void pin4(u32x4& x) { PIN(x); }
__device__ __forceinline__ void pinf(float& x) { PIN(x); }
#define HCAP 5
__device__ __forceinline__ void pace() { asm volatile("s_waitcnt vmcnt(%0)" :: "i"(HCAP) : "memory"); if (PACE) __builtin_amdgcn_s_sleep(PACE); }
template <int V> struct IC { static constexpr int value = V; };
template <int I, int N, class F>
__device__ __forceinline__ void static_for(F&& f) {
  if constexpr (I < N) { f(IC<I>{}); static_for<I + 1, N>(f); }
}

struct Ctx {
  lptr lds; unsigned lbase;
  char* ws;
  rsrc_t rws;
  unsigned tag, map, home;
  char* wb;
  int lane, w8, w, r, h;
};

__device__ __forceinline__ constexpr unsigned spread(unsigned L) { return L + (L & ~8191u); }
__device__ __forceinline__ unsigned base_a(const Ctx& c, unsigned g) { return OFF_A + (g << 21) + (((c.map >> g) & 1u) << 13); }
__device__ __forceinline__ unsigned base_b(const Ctx& c, unsigned g) { return OFF_B + (g << 20) + (((c.map >> (8 + g)) & 1u) << 13); }
__device__ __forceinline__ void stampf(const Ctx& c, int kid, int i) { STAMP(kid, i); }
__device__ __forceinline__ void ev_set(const Ctx& c, int idx, int word, unsigned one = 1) {
  const unsigned addr = c.lbase + L_CNT + idx * 16 + word * 4;
  asm volatile("s_mov_b64 exec, 1\n\tds_write_b32 %0, %1\n\ts_mov_b64 exec, -1" :: "v"(addr), "v"(one) : "memory");
}
template <int NWORDS, int SLEEP = 0>
__device__ __forceinline__ void ev_wait(const Ctx& c, int idx) {
  const unsigned addr = c.lbase + L_CNT + idx * 16;
#pragma unroll 1
  for (int spins = 0; spins < (1 << 24); ++spins) {
    u32x4 v;
    asm volatile("ds_read_b128 %0, %1\n\ts_waitcnt lgkmcnt(0)" : "=v"(v) : "v"(addr) : "memory");
    if (__builtin_amdgcn_readfirstlane(NWORDS == 4 ? (v[0] & v[1] & v[2] & v[3]) : v[0])) break;
    if (SLEEP) __builtin_amdgcn_s_sleep(SLEEP);
  }
}
template <int SLEEP = 0>
__device__ __forceinline__ void ev_wait_half(const Ctx& c, int idx) {
  const unsigned addr = c.lbase + L_CNT + idx * 16 + (c.w >> 1) * 8;
#pragma unroll 1
  for (int spins = 0; spins < (1 << 24); ++spins) {
    u32x2 v;
    asm volatile("ds_read_b64 %0, %1\n\ts_waitcnt lgkmcnt(0)" : "=v"(v) : "v"(addr) : "memory");
    if (__builtin_amdgcn_readfirstlane(v[0] & v[1])) break;
    if (SLEEP) __builtin_amdgcn_s_sleep(SLEEP);
  }
}
template <int NW>
__device__ __forceinline__ void publish(const Ctx& c, int idx, unsigned flag_off) {
  const unsigned addr = c.lbase + L_CNT + idx * 16, one = 1;
  unsigned old;
  asm volatile("s_waitcnt vmcnt(0)\n\ts_mov_b64 exec, 1\n\tds_add_rtn_u32 %0, %1, %2\n\ts_mov_b64 exec, -1\n\ts_waitcnt lgkmcnt(0)"
               : "=v"(old) : "v"(addr), "v"(one) : "memory");
  if (__builtin_amdgcn_readfirstlane(old) == NW - 1) {
    const unsigned tag = c.tag;
    asm volatile("s_mov_b64 exec, 1\n\tbuffer_store_dword %0, off, %1, %2 sc1\n\ts_mov_b64 exec, -1" :: "v"(tag), "s"(c.rws), "s"(flag_off) : "memory");
  }
}
__device__ __forceinline__ unsigned poll(const Ctx& c, unsigned voff) {
  unsigned f;
#pragma unroll 1
  for (int spins = 0; spins < (1 << 17); ++spins) {
    asm volatile("buffer_load_dword %0, %1, %2, 0 offen sc1\n\ts_waitcnt vmcnt(0)" : "=v"(f) : "v"(voff), "s"(c.rws) : "memory");
    if (__all((f ^ c.tag) < 2u)) break;
  }
  return f;
}
__device__ __forceinline__ void acquire(const Ctx& c, unsigned voff, int ev) {
  if (c.w == 0) { poll(c, voff); ev_set(c, ev, 0); }
  else ev_wait<1>(c, ev);
}
__device__ __forceinline__ unsigned flag1_off(int idx) { return OFF_F1 + spread((unsigned)idx * 256u); }
__device__ __forceinline__ unsigned flag_off(const Ctx& c, int phase, int idx, unsigned grp) { return base_a(c, grp) + spread(POS_FL + (phase - 1) * 8192u) + (idx & 31) * 256u; }
__device__ __forceinline__ void touch_lds(const Ctx& c, rsrc_t r, unsigned voff, unsigned soff) {
  asm volatile("s_mov_b32 m0, %0\n\ts_nop 0\n\tbuffer_load_dword %1, %2, %3 offen lds" :: "s"(c.lbase + L_DUMMY), "v"(voff), "s"(r), "s"(soff) : "memory");
}

template <int CTRL>
__device__ __forceinline__ float dpp_add(float v) {
  return v + __int_as_float(__builtin_amdgcn_update_dpp(0, __float_as_int(v), CTRL, 0xf, 0xf, true));
}
__device__ __forceinline__ float half_sum(float v) {
  v = dpp_add<0x140>(dpp_add<0x141>(dpp_add<0x4E>(dpp_add<0xB1>(v))));
  auto r = __builtin_amdgcn_permlane16_swap(__float_as_uint(v), __float_as_uint(v), false, false);
  return __uint_as_float(r[0]) + __uint_as_float(r[1]);
}

__device__ __forceinline__ void p1_stats(Ctx& c, const char* x, const char* wqkv, const char* wo, const char* w1, const char* w2, const char* out, int rb, int cb) {
  const int w = c.w, r = c.r;
  const int row = rb * 128 + cb * 8 + w * 2 + c.h;
  u32x4 xs0, xs1;
  const unsigned vo = r < 28 ? (unsigned)row * 896u + r * 32u : OOB;
  ld16p(xs0, mkrs(x, S_ * D_ * 2), vo, 0); ld16p(xs1, mkrs(x, S_ * D_ * 2), vo, 16);
  touch_lds(c, mkrs(wqkv, D_ * 3 * D_ * 2), c.lane < 10 ? c.lane * 128u : OOB, (blockIdx.x * 4 + w) * 1176u);
  {
    const unsigned b = blockIdx.x, g = b & 7, j = b >> 3, i = c.lane >> 1;
    const unsigned fl = w == 0 ? flag1_off(cb * 16 + rb)
                      : OFF_A + ((w == 1 ? b >> 5 : g) << 21) + spread(POS_FL + (w - 1) * 8192u) +
                        (w == 1 ? ((b >> 4) & 1) * 16 + g * 2 + (j & 1) : (w == 2 ? (j & 3) * 8 + (j >> 2) : (j & 1) * 16 + (j >> 1))) * 256u;
    const unsigned a = i == 0 ? (unsigned)((cb * 3 + 1) * 16 + rb) * 16384u + w * 256u : (i == 1 ? fl : (w == 0 ? OFF_LN + (rb * 128 + cb * 8) * 16u : OFF_PR));
    const char* wt = w == 0 ? wo + b * 1568u : (w == 1 ? w1 + b * 6272u : (w == 2 ? w2 + b * 6272u : out + b * 7168u));
    const char* p = c.lane < 5 ? c.wb + (a + (c.lane & 1) * 8192u) : wt;
    asm volatile("s_mov_b32 m0, %0\n\ts_mov_b64 exec, 63\n\tglobal_load_lds_dword %1, off\n\ts_mov_b64 exec, -1" :: "s"(c.lbase + L_DUMMY), "v"(p) : "memory");
  }
  if (threadIdx.x < EV_COUNT * 4) *(LAS unsigned*)(c.lds + L_CNT + threadIdx.x * 4) = 0;
  unsigned prev;
  asm volatile("s_load_dword %0, %1, 0x0\n\ts_waitcnt lgkmcnt(0)" : "=s"(prev) : "s"(c.wb + OFF_EP) : "memory");
  c.tag = (prev * 0x9E3779B1u + 0x7F4A7C15u) & ~1u;
  static_assert(EV_TAG < 16, "zeroed and set by wave 0");
  if (w == 0) ev_set(c, EV_TAG, 0, c.tag);
  asm volatile("s_waitcnt vmcnt(2)" : "+v"(xs0), "+v"(xs1) :: "memory");
  float s0 = 0.f, s1 = 0.f, q0 = 0.f, q1 = 0.f;
#pragma unroll
  for (int e = 0; e < 4; ++e) { s0 = dot2(xs0[e], ONES2, s0); s1 = dot2(xs1[e], ONES2, s1); q0 = dot2(xs0[e], xs0[e], q0); q1 = dot2(xs1[e], xs1[e], q1); }
  const float mu = half_sum(s0 + s1) * (1.f / 448.f);
  const float rstd = __builtin_amdgcn_rsqf(fmaxf(half_sum(q0 + q1) * (1.f / 448.f) - mu * mu, 0.f) + EPS);
  st16<0>((u32x4){__float_as_uint(-mu), c.tag, __float_as_uint(rstd), c.tag}, c.rws, r == 0 ? (unsigned)row * 16u : OOB, OFF_LN);
}

template <int NQ>
__device__ __forceinline__ void p1_compute(Ctx& c, int rb, int cb) {
  const int lane = c.lane, w = c.w, r = c.r, h = c.h;
  lptr lds = c.lds;
  const int arow = w * 32 + r;
  const unsigned aoff = arow * 128 + ((h ^ ((arow >> 1) & 7)) * 16);
  const unsigned boff = 16384 + (8 * h + ((lane & 15) >> 2)) * 192 + ((lane >> 4) & 1) * 32 + (lane & 3) * 8 + (cb & 1) * 8;
  unsigned ao[4], bo[4];
#pragma unroll
  for (int i = 0; i < 4; ++i) { const int ks = (w + i) & 3; ao[i] = aoff ^ (ks * 32); bo[i] = boff + ks * 3072; }
  f32x16 acc[3] = {};
  float cs[3] = {0.f, 0.f, 0.f};
  u32x4 a[4], b[4][3];
  auto dots = [&]() {
#pragma unroll
    for (int e = 0; e < 4; ++e)
#pragma unroll
      for (int n = 0; n < 3; ++n) cs[n] = dot2(b[0][n][e], ONES2, cs[n]);
    PIN(cs[0]); PIN(cs[1]); PIN(cs[2]);
    SBAR();
  };
  BAR();
  STAMP(0, 1);
#pragma unroll
  for (int i = 0; i < 4; ++i) {
#pragma unroll
    for (int n = 0; n < 3; ++n) b[i][n] = ldstr2(lds + bo[i] + n * 56, 768);
    a[i] = lds128(lds + ao[i]);
  }
  SBAR();
#pragma unroll 1
  for (int st = 1; st < 7; ++st) {
    lptr sb = lds + (st >= 5 ? st - 5 : st) * P1_SLOT;
    dots();
    asm volatile("s_barrier" ::: "memory");
#pragma unroll
    for (int i = 0; i < 4; ++i)
#pragma unroll
      for (int n = 0; n < 3; ++n) {
        acc[n] = n < NQ ? MFMA(frag(b[i][n]), frag(a[i]), acc[n]) : MFMA(frag(a[i]), frag(b[i][n]), acc[n]);
        SBAR();
        b[i][n] = ldstr2(sb + bo[i] + n * 56, 768);
        if (n == 2) a[i] = lds128(sb + ao[i]);
        SBAR();
      }
  }
  STAMP(0, 7);
  dots();
#pragma unroll
  for (int i = 0; i < 2; ++i)
#pragma unroll
    for (int n = 0; n < 3; ++n) {
      acc[n] = n < NQ ? MFMA(frag(b[i][n]), frag(a[i]), acc[n]) : MFMA(frag(a[i]), frag(b[i][n]), acc[n]);
      if (i == 0) {
        const float t = xhalf_sum(cs[n]);
        *(LAS float*)(lds + L_CS + (n * 32 + r) * 16 + w * 4) = t;
      }
      SBAR();
    }
  BAR();
  f32x4 p4[3], r4[4];
  u32x4 cf[3];
#pragma unroll
  for (int n = 0; n < 3; ++n) p4[n] = ldsf4(lds + L_CS + (n * 32 + r) * 16);
  const u32x4 gap = lds128(lds + L_CNT + EV_GAP * 16);
  lptr srow = lds + L_ROW + w * 128;
  const float rstd = *(LAS float*)(srow + r * 4), nmu = *(LAS float*)(srow + 512 + r * 4);
  if (NQ < 3) {
#pragma unroll
    for (int g = 0; g < 4; ++g) r4[g] = ldsf4(srow + (8 * g + 4 * h) * 4);
  }
  u32x4 rf;
  SBAR();
#pragma unroll
  for (int i = 2; i < 4; ++i)
#pragma unroll
    for (int n = 0; n < 3; ++n) {
      acc[n] = n < NQ ? MFMA(frag(b[i][n]), frag(a[i]), acc[n]) : MFMA(frag(a[i]), frag(b[i][n]), acc[n]);
      if (i == 2 && n == 2) { rf = rowfrag(nmu); PIN(rf); }
      if (i == 3) {
        float t0 = p4[n][0] + p4[n][1], t1 = p4[n][2] + p4[n][3];
        PIN(t0); PIN(t1);
        cf[n] = colfrag(t0 + t1, h);
        PIN(cf[n]);
      }
      SBAR();
    }
#pragma unroll
  for (int n = 0; n < 3; ++n) acc[n] = n < NQ ? MFMA(frag(cf[n]), frag(rf), acc[n]) : MFMA(frag(rf), frag(cf[n]), acc[n]);
  STAMP(0, 8);
  c.home = __builtin_amdgcn_readfirstlane(gap[0] + gap[2] < gap[1] + gap[3] ? 1u : 0u);
  const unsigned vk = ((lane & 0x33) | ((lane & 4) << 1) | ((lane & 8) >> 1)) * 16;
#pragma unroll
  for (int n = 0; n < 3; ++n) {
    const int u = cb * 3 + n;
    const float sc = u < 16 ? rstd * QSCALE : rstd;
#pragma unroll
    for (int s = 0; s < 2; ++s) {
      u32x4 o;
#pragma unroll
      for (int i = 0; i < 4; ++i) {
        const int e = 8 * s + 2 * i;
        o[i] = n < NQ ? pk2(acc[n][e] * sc, acc[n][e + 1] * sc) : pk2(acc[n][e] * r4[e >> 2][e & 3], acc[n][e + 1] * r4[e >> 2][(e & 3) + 1]);
      }
      if (n < NQ) {
        if (s == 1 && h) { o[2] = u < 16 ? 0u : ONES2; o[3] = u < 16 ? 0u : 0x00003F80u; }
      } else if (r >= 28) {
        o = (u32x4){ONES2, ONES2, ONES2, ONES2};
      }
      st16s(s, o, c.rws, (n < NQ && u >= 16) ? vk : lane * 16, (unsigned)(u * 16 + rb) * 16384u + c.home * 8192u + w * 2048u);
    }
  }
  STAMP(0, 9);
}

__device__ __forceinline__ void p1_load(Ctx& c, const char* x, const char* wqkv, int rb, int cb) {
  const int lane = c.lane, w = c.w;
  const rsrc_t rx = mkrs(x, S_ * D_ * 2), rw = mkrs(wqkv, D_ * 3 * D_ * 2);
  unsigned vxa[2], vw[3];
#pragma unroll
  for (int p = 0; p < 2; ++p) vxa[p] = (unsigned)(lane >> 3) * 896u + (unsigned)((lane & 7) ^ ((4 * p + (lane >> 4)) & 7)) * 16u;
#pragma unroll
  for (int q = 0; q < 3; ++q) { const int i = q * 64 + lane; vw[q] = (unsigned)(i / 12) * 2688u + (unsigned)(i % 12) * 16u; }
  const unsigned sx0 = (unsigned)(rb * 128 + w * 32) * 896u, sw0 = (((unsigned)cb * 168u) & ~15u) + (unsigned)w * (16u * 2688u);
#pragma unroll 1
  for (int st = 0; st < 7; ++st) {
    const unsigned lb = c.lbase + (st >= 5 ? st - 5 : st) * P1_SLOT;
#pragma unroll
    for (int p = 0; p < 4; ++p) dma16(rx, vxa[p & 1], sx0 + st * 128u + p * 7168u, lb + (w * 4 + p) * 1024);
#pragma unroll
    for (int q = 0; q < 3; ++q) dma16(rw, vw[q], sw0 + st * (64u * 2688u), lb + 16384 + (w * 3 + q) * 1024);
    if (st && st < 6) WAITV_BAR(7);
  }
  u32x4 ln;
  const unsigned vln = (unsigned)(rb * 128 + w * 32 + c.r) * 16u;
  ld16<0>(ln, c.rws, vln, OFF_LN);
  WAITV_BAR(8);
  asm volatile("s_waitcnt vmcnt(0)\n\ts_barrier" : "+v"(ln) :: "memory");
  c.tag = __builtin_amdgcn_readfirstlane(*(volatile LAS unsigned*)(c.lds + L_CNT + EV_TAG * 16));
#pragma unroll 1
  for (int spins = 0; spins < (1 << 17) && !__all(ln[1] == c.tag && ln[3] == c.tag); ++spins)
    asm volatile("buffer_load_dwordx4 %0, %1, %2, %3 offen sc1\n\ts_waitcnt vmcnt(0)" : "=v"(ln) : "v"(vln), "s"(c.rws), "s"(OFF_LN) : "memory");
  *(LAS unsigned*)(c.lds + L_ROW + c.h * 512 + w * 128 + c.r * 4) = c.h ? ln[0] : ln[2];
  {
    const unsigned so = (unsigned)((cb * 3 + 1) * 16 + rb) * 16384u + w * 256u + (w & 1) * 8192u, z = 0;
    unsigned long long t0, t1;
    asm volatile("buffer_store_dword %2, off, %3, %4 sc1\n\tbuffer_store_dword %2, off, %3, %5 sc1\n\ts_waitcnt vmcnt(1)\n\ts_memtime %0\n\t"
                 "s_waitcnt vmcnt(0)\n\ts_memtime %1\n\ts_waitcnt lgkmcnt(0)"
                 : "=&s"(t0), "=&s"(t1) : "v"(z), "s"(c.rws), "s"(so), "s"(so ^ 8192u) : "memory");
    ev_set(c, EV_GAP, w, (unsigned)(t1 - t0));
  }
  BAR();
}

#define SMF(v, p, c) __builtin_amdgcn_smfmac_f32_32x32x32_bf16(__builtin_bit_cast(bf16x8n, v), __builtin_bit_cast(bf16x16n, p), (c), sidx, 0, 0)
#define LO8(S) __builtin_shufflevector(S, S, 0, 1, 2, 3, 4, 5, 6, 7)
#define ATT_UNIT(SC, SN, KN0, KN1, QN, OPREV, VPREV1, OCUR, VCUR0, HOOK_A, HOOK_B, HOOK_C, HOOK_D)       \
  do {                                                                                                     \
    SN = MFMA(frag(KN0), frag(QN[0]), zero); SBAR();                                                       \
    SC[0] = EX(SC[0]); SC[1] = EX(SC[1]); SC[2] = EX(SC[2]); SC[3] = EX(SC[3]);                            \
    PIN(SC); SBAR(); HOOK_A;                                                                               \
    SN = MFMA(frag(KN1), frag(QN[1]), SN); SBAR();                                                         \
    SC[4] = EX(SC[4]); SC[5] = EX(SC[5]); SC[6] = EX(SC[6]); SC[7] = EX(SC[7]);                            \
    PIN(SC); SBAR(); HOOK_B;                                                                               \
    OPREV = SMF(VPREV1, e, OPREV); SBAR();                                                                 \
    e[0] = EX(SC[8]); e[1] = EX(SC[9]); e[2] = EX(SC[10]); e[3] = EX(SC[11]);                              \
    PIN(e); SBAR(); HOOK_C;                                                                                \
    OCUR = SMF(VCUR0, LO8(SC), OCUR); SBAR();                                                              \
    e[4] = EX(SC[12]); e[5] = EX(SC[13]); e[6] = EX(SC[14]); e[7] = EX(SC[15]);                            \
    PIN(e); SBAR(); HOOK_D;                                                                                \
  } while (0)

__device__ __forceinline__ int p4_rot(int cb4) { return cb4 >= 7 ? cb4 - 7 : cb4; }
#define P5ROT (cb3 & 1) * 6
__device__ __forceinline__ int p5_rot(int cb3) { return P5ROT; }
__device__ __forceinline__ unsigned w1_piece(int p, int cb4) {
  const int q = p + p4_rot(cb4) * 16;
  return (unsigned)(q >= 112 ? q - 112 : q) * 14336u + cb4 * 256u;
}
struct XchgIn { u32x4 f; f32x4 m, v[3][4]; unsigned map; };
__device__ __forceinline__ bool l_ok(float l) { return l >= 0x1p-30f && l <= 0x1p64f; }
template <bool EXACT>
__device__ __forceinline__ void attn_pass(const Ctx& c, unsigned hm, unsigned hq, bool& sent, f32x16& own, float& mown, XchgIn& in) {
  f32x16 o[4];
  float m[4];
  const int lane = c.lane, w = c.w, h = c.h;
  const int hd = (blockIdx.x & 7) * 2 + ((blockIdx.x >> 3) & 1), rb = blockIdx.x >> 4;
  const unsigned vo = lane * 16;
  const unsigned sq = (unsigned)(hd * 16 + rb) * 16384u + hq * 8192u;
  const unsigned sk = (unsigned)((16 + hd) * 16 + w * 4) * 16384u + (rb & 4) * 1024u, hw = hm >> (w * 4);
  auto kblk = [&](int i) { const unsigned x = (i + rb) & 3; return sk + x * 16384u + ((hw >> x) & 1u) * 8192u; };
  auto vblk = [&](int i) { const unsigned x = (i + rb) & 3; return sk + (256u + x) * 16384u + ((hw >> (16 + x)) & 1u) * 8192u; };
  const unsigned kr0 = kblk(0), vr0 = vblk(0), kr1 = kr0 ^ 4096u, vr1 = vr0 ^ 4096u;
  const rsrc_t rs = c.rws;
  const f32x16 zero = {};
  u32x4 q0[2], q1[2], q2[2], q3[2], ka0, ka1, va0, va1, kb0, kb1, vb0, vb1, kc0, kc1, vc0, vc1, kd0, kd1, vd0, vd1;
  unsigned sqb[4];
#pragma unroll
  for (int b = 0; b < 4; ++b) { m[b] = -INFINITY; sqb[b] = sq + ((w + 1 + b) & 3) * 2048u; }
  f32x8 e;
  int sidx = 0xDDDD;
  PIN(sidx);
#pragma unroll 1
  for (int i = EXACT ? 15 : 0; i >= 0; --i) {
    const unsigned ko = kblk(i >> 2) ^ ((i & 3) * 2048u);
    ld16<0>(ka0, rs, vo, ko); ld16<0>(q0[0], rs, vo, sqb[0]); ld16<1024>(ka1, rs, vo, ko); ld16<1024>(q0[1], rs, vo, sqb[0]);
    ld16<0>(q1[0], rs, vo, sqb[1]); ld16<1024>(q1[1], rs, vo, sqb[1]); ld16<0>(q2[0], rs, vo, sqb[2]); ld16<1024>(q2[1], rs, vo, sqb[2]);
    ld16<0>(q3[0], rs, vo, sqb[3]); ld16<1024>(q3[1], rs, vo, sqb[3]);
#pragma unroll
    for (int b = 0; b < 4; ++b) { o[b] = zero; pin16(o[b]); }
    vd1 = (u32x4){}; e = f32x8{};
    PIN(vd1); PIN(e);
    asm volatile("s_waitcnt vmcnt(0)" : "+v"(q0[0]), "+v"(q0[1]), "+v"(q1[0]), "+v"(q1[1]), "+v"(q2[0]), "+v"(q2[1]), "+v"(q3[0]), "+v"(q3[1]),
                 "+v"(ka0), "+v"(ka1) :: "memory");
    f32x16 s0 = MFMA(frag(ka0), frag(q0[0]), zero), s1 = MFMA(frag(ka0), frag(q1[0]), zero);
    s0 = MFMA(frag(ka1), frag(q0[1]), s0); s1 = MFMA(frag(ka1), frag(q1[1]), s1);
    f32x16 s2 = MFMA(frag(ka0), frag(q2[0]), zero), s3 = MFMA(frag(ka0), frag(q3[0]), zero);
    s2 = MFMA(frag(ka1), frag(q2[1]), s2); s3 = MFMA(frag(ka1), frag(q3[1]), s3);
    ld16<0>(va0, rs, vo, vr0);
    ld16<1024>(va1, rs, vo, vr0); ld16<2048>(kb0, rs, vo, kr0); ld16<3072>(kb1, rs, vo, kr0); ld16<2048>(vb0, rs, vo, vr0);
#pragma unroll
    for (int e_ = 0; e_ < 16; ++e_) { const int e = e_; m[0] = fmaxf(m[0], s0[e]); m[1] = fmaxf(m[1], s1[e]); m[2] = fmaxf(m[2], s2[e]); m[3] = fmaxf(m[3], s3[e]); }
  }
#pragma unroll
  for (int b = 0; b < 4; ++b) {
    m[b] = xhalf_max(m[b]);
    const float nm = -m[b];
    const unsigned u0 = __float_as_uint(nm) & 0xffff0000u;
    const float r1 = nm - __uint_as_float(u0);
    const unsigned u1 = __float_as_uint(r1) & 0xffff0000u;
    const float r2 = r1 - __uint_as_float(u1);
    u32x4& qq = b == 0 ? q0[1] : (b == 1 ? q1[1] : (b == 2 ? q2[1] : q3[1]));
    qq[2] = h ? ((u0 >> 16) | u1) : qq[2];
    qq[3] = h ? (__float_as_uint(r2) >> 16) : qq[3];
  }
  STAMP(1, 1);
  f32x16 sa = MFMA(frag(ka0), frag(q0[0]), zero), sb;
  sa = MFMA(frag(ka1), frag(q0[1]), sa);
  SBAR();
  ld16<3072>(vb1, rs, vo, vr0); ld16<0>(kc0, rs, vo, kr1); ld16<1024>(kc1, rs, vo, kr1); ld16<0>(vc0, rs, vo, vr1);
  ld16<1024>(vc1, rs, vo, vr1); ld16<2048>(kd0, rs, vo, kr1); ld16<3072>(kd1, rs, vo, kr1); ld16<2048>(vd0, rs, vo, vr1);
  unsigned vc = vr1;
#define ATT_TILE(K0, K1, V0, V1, KN0, KN1, VP1, VP1_OFF, VP1_S, OFF, KS, VS)                                              \
    ATT_UNIT(sa, sb, K0, K1, q1, o[3], VP1, o[0], V0, (void)0, (void)0,                                                   \
             asm volatile("s_waitcnt vmcnt(11)" : "+v"(V0), "+v"(V1) :: "memory"), ld16<VP1_OFF>(VP1, rs, vo, VP1_S));    \
    ATT_UNIT(sb, sa, K0, K1, q2, o[0], V1, o[1], V0, (void)0, (void)0, (void)0, (void)0);                                 \
    ATT_UNIT(sa, sb, K0, K1, q3, o[1], V1, o[2], V0, ld16<OFF>(K0, rs, vo, KS), (void)0, ld16<OFF + 1024>(K1, rs, vo, KS), \
             asm volatile("s_waitcnt vmcnt(12)" : "+v"(KN0), "+v"(KN1) :: "memory"));                                     \
    ATT_UNIT(sb, sa, KN0, KN1, q0, o[2], V1, o[3], V0, (void)0, (void)0, (void)0, ld16<OFF>(V0, rs, vo, VS))
#pragma unroll 1
  for (int i = 0; i < 3; ++i) {
    const unsigned k0 = kblk(i + 1), k1 = k0 ^ 4096u, v0 = vblk(i + 1), v1 = v0 ^ 4096u;
    ATT_TILE(ka0, ka1, va0, va1, kb0, kb1, vd1, 3072, vc, 0, k0, v0);
    ATT_TILE(kb0, kb1, vb0, vb1, kc0, kc1, va1, 1024, v0, 2048, k0, v0);
    ATT_TILE(kc0, kc1, vc0, vc1, kd0, kd1, vb1, 3072, v0, 0, k1, v1);
    ATT_TILE(kd0, kd1, vd0, vd1, ka0, ka1, vc1, 1024, v1, 2048, k1, v1);
    vc = v1;
  }
  unsigned l16 = vo;
  PIN(l16);
  lptr xa[3], xin = c.lds + L_XA + w * 12288 + l16;
#pragma unroll
  for (int b = 0; b < 3; ++b) xa[b] = c.lds + L_XA + (((w + 1 + b) & 3) * 3 + b) * 4096 + l16;
#define XA_OUT(B, G) do { if (!EXACT || !sent) *(LAS f32x4*)(xa[B] + (G) * 1024) = __builtin_shufflevector(o[B], o[B], 4 * (G), 4 * (G) + 1, 4 * (G) + 2, 4 * (G) + 3); SBAR(); } while (0)
#define XA_OUT4(B) XA_OUT(B, 0), XA_OUT(B, 1), XA_OUT(B, 2), XA_OUT(B, 3)
#define XA_IN(B, G) do { in.v[B][G] = *(volatile LAS f32x4*)(xin + (B) * 4096 + (G) * 1024); } while (0)
#define XA_IN2(B, G) do { XA_IN(B, G); XA_IN(B, G + 1); SBAR(); } while (0)
#define NOHOOK (void)0, (void)0, (void)0, (void)0
#define WV(N, A, B) asm volatile("s_waitcnt vmcnt(" #N ")" : "+v"(A), "+v"(B) :: "memory")
#define UNIT(...) ATT_UNIT(__VA_ARGS__)
  UNIT(sa, sb, ka0, ka1, q1, o[3], vd1, o[0], va0, (void)0, (void)0, WV(11, va0, va1), ld16<3072>(vd1, rs, vo, vc));
  UNIT(sb, sa, ka0, ka1, q2, o[0], va1, o[1], va0, (void)0, (void)0, (void)0, WV(10, kb0, kb1));
  UNIT(sa, sb, kb0, kb1, q0, o[1], va1, o[2], va0, NOHOOK);
  UNIT(sb, sa, kb0, kb1, q1, o[2], va1, o[0], vb0, (void)0, (void)0, WV(8, vb0, vb1), (void)0);
  UNIT(sa, sb, kb0, kb1, q2, o[0], vb1, o[1], vb0, (void)0, (void)0, (void)0, WV(6, kc0, kc1));
  UNIT(sb, sa, kc0, kc1, q0, o[1], vb1, o[2], vb0, NOHOOK);
  UNIT(sa, sb, kc0, kc1, q1, o[2], vb1, o[0], vc0, (void)0, (void)0, WV(4, vc0, vc1), (void)0);
  UNIT(sb, sa, kc0, kc1, q2, o[0], vc1, o[1], vc0, (void)0, (void)0, (void)0, WV(2, kd0, kd1));
  UNIT(sa, sb, kd0, kd1, q0, o[1], vc1, o[2], vc0, NOHOOK);
  UNIT(sb, sa, kd0, kd1, q1, o[2], vc1, o[0], vd0, (void)0, (void)0, WV(0, vd0, vd1), (void)0);
  UNIT(sa, sb, kd0, kd1, q2, o[0], vd1, o[1], vd0, NOHOOK);
  UNIT(sb, sa, ka0, ka1, q3, o[1], vd1, o[2], vd0, XA_OUT4(0));
  UNIT(sa, sb, kb0, kb1, q3, o[2], vd1, o[3], va0, XA_OUT4(1));
  UNIT(sb, sa, kc0, kc1, q3, o[3], va1, o[3], vb0, XA_OUT4(2));
  {
    const bool ok = EXACT || h == 0 || (l_ok(o[0][12]) && l_ok(o[1][12]) && l_ok(o[2][12]));
    const bool go = !sent && __all(ok);
#pragma unroll
    for (int b = 0; b < 3; ++b) *(LAS float*)(c.lds + ((sent || h) ? L_CS + lane * 4 : L_ML + ((w + 1 + b) & 3) * 512 + c.r * 16 + b * 4)) = m[b];
    ev_set(c, EV_XA, w, (go || sent) ? 1u : 0u);
    sent = sent || go;
    SBAR();
  }
  UNIT(sa, sb, kd0, kd1, q3, o[3], vb1, o[3], vc0, NOHOOK);
  in.f = *(volatile LAS u32x4*)(c.lds + L_CNT + EV_XA * 16);
  in.map = *(volatile LAS unsigned*)(c.lds + L_CNT + EV_MAP * 16);
  in.m = *(volatile LAS f32x4*)(c.lds + L_ML + w * 512 + c.r * 16);
  SBAR();
  UNIT(sb, sa, kd0, kd1, q3, o[3], vc1, o[3], vd0, { XA_IN2(0, 0); XA_IN(0, 2); }, { XA_IN(0, 3); XA_IN2(1, 0); }, { XA_IN2(1, 2); XA_IN(2, 0); }, { XA_IN(2, 1); XA_IN2(2, 2); });
  own = SMF(vd1, e, o[3]);
  mown = m[3];
}

__device__ __forceinline__ void p2_attn(Ctx& c) {
  const int lane = c.lane, w = c.w, r = c.r, h = c.h;
  lptr lds = c.lds;
  const int hd = (blockIdx.x & 7) * 2 + ((blockIdx.x >> 3) & 1), rb = blockIdx.x >> 4;
  unsigned hm, hq;
  {
    const int u = lane < 16 ? 16 + hd : (lane < 32 ? 32 + hd : hd);
    if (w == 0) {
      const unsigned long long bm = __ballot(poll(c, flag1_off((u / 3) * 16 + (lane < 32 ? (lane & 15) : rb)) + (c.home ^ (hd >> 3)) * 8192u) & 1u);
      hm = (unsigned)bm; hq = (unsigned)(bm >> 32) & 1u;
      ev_set(c, EV_GO2, 1, hm); ev_set(c, EV_GO2, 0, 1u + hq);
    } else {
      const unsigned addr = c.lbase + L_CNT + EV_GO2 * 16;
#pragma unroll 1
      for (int spins = 0; spins < (1 << 24); ++spins) {
        u32x2 v;
        asm volatile("ds_read_b64 %0, %1\n\ts_waitcnt lgkmcnt(0)" : "=v"(v) : "v"(addr) : "memory");
        hq = __builtin_amdgcn_readfirstlane(v[0]); hm = __builtin_amdgcn_readfirstlane(v[1]);
        if (hq) break;
      }
      hq -= 1;
    }
  }
  STAMP(1, 0);
  f32x16 own;
  float mown;
  XchgIn in;
  bool sent = false;
  __builtin_amdgcn_s_setprio(3);
  attn_pass<false>(c, hm, hq, sent, own, mown, in);
  if (!sent || !__all(h == 0 || l_ok(own[12]))) attn_pass<true>(c, hm, hq, sent, own, mown, in);
  STAMP(1, 2);
  c.map = __builtin_amdgcn_readfirstlane(in.map);
  if (!__builtin_amdgcn_readfirstlane(in.f[0] & in.f[1] & in.f[2] & in.f[3]) || !c.map) {
    pin16(own);
    ev_wait<4>(c, EV_XA);
    ev_wait<1>(c, EV_MAP);
    c.map = __builtin_amdgcn_readfirstlane(*(volatile LAS unsigned*)(lds + L_CNT + EV_MAP * 16));
    in.m = *(volatile LAS f32x4*)(lds + L_ML + w * 512 + r * 16);
#pragma unroll
    for (int b = 0; b < 3; ++b)
#pragma unroll
      for (int g = 0; g < 4; ++g) in.v[b][g] = *(volatile LAS f32x4*)(lds + L_XA + w * 12288 + lane * 16 + b * 4096 + g * 1024);
  }
  STAMP(1, 4);
  const float M = fmaxf(fmaxf(in.m[0], in.m[1]), fmaxf(in.m[2], mown));
  const float wk[4] = {EX(in.m[0] - M), EX(in.m[1] - M), EX(in.m[2] - M), EX(mown - M)};
  f32x4 f[4];
#pragma unroll
  for (int g = 3; g >= 0; --g) {
    const f32x4 ow = {own[4 * g], own[4 * g + 1], own[4 * g + 2], own[4 * g + 3]};
    f[g] = (in.v[0][g] * wk[0] + in.v[1][g] * wk[1]) + (in.v[2][g] * wk[2] + ow * wk[3]);
  }
  const float inv = __builtin_amdgcn_rcpf(__uint_as_float(__builtin_amdgcn_permlane32_swap(__float_as_uint(f[3][0]), __float_as_uint(f[3][0]), false, false)[1]));
  u32x4 res[2];
#pragma unroll
  for (int g = 0; g < 4; ++g) { res[g >> 1][(g & 1) * 2] = pk2(f[g][0] * inv, f[g][1] * inv); res[g >> 1][(g & 1) * 2 + 1] = pk2(f[g][2] * inv, f[g][3] * inv); }
  if (h) { res[1][2] = 0; res[1][3] = 0; }
#pragma unroll
  for (int s = 0; s < 2; ++s) st16s(s, res[s], c.rws, lane * 16, base_b(c, rb >> 1) + spread(POS_O + (unsigned)(((rb & 1) * 4 + w) * 16 + hd) * 2048u));
  STAMP(1, 5);
}
__device__ __forceinline__ void p2_publish(const Ctx& c) {
  publish<4>(c, EV_PUB2, flag_off(c, 1, (blockIdx.x >> 4) * 16 + (blockIdx.x & 7) * 2 + ((blockIdx.x >> 3) & 1), blockIdx.x >> 5));
}

__device__ __forceinline__ void stats_issue(const Ctx& c, int t, f32x2 (&v)[7]) {
  const unsigned vo = c.h * 1792 + c.r * 8, so = base_a(c, blockIdx.x & 7) + spread(POS_ST + (t & 7) * 4096u);
#pragma unroll
  for (int i = 0; i < 7; ++i)
    asm volatile("buffer_load_dwordx2 %0, %1, %2, %3 offen offset:%4 sc1" : "=v"(v[i]) : "v"(vo), "s"(c.rws), "s"(so), "i"(i * 256) : "memory");
}
#define STATS_WAIT(N, v) asm volatile("s_waitcnt vmcnt(" #N ")" : "+v"(v[0]), "+v"(v[1]), "+v"(v[2]), "+v"(v[3]), "+v"(v[4]), "+v"(v[5]), "+v"(v[6]) :: "memory")
__device__ __forceinline__ void stats_pin(f32x2 (&v)[7]) { asm volatile("" : "+v"(v[0]), "+v"(v[1]), "+v"(v[2]), "+v"(v[3]), "+v"(v[4]), "+v"(v[5]), "+v"(v[6])); }
__device__ __forceinline__ void stats_finish(const f32x2 (&v)[7], float& mu, float& rstd) {
  const f32x2 t = ((v[0] + v[1]) + (v[2] + v[3])) + ((v[4] + v[5]) + v[6]);
  mu = xhalf_sum(t[0]) * (1.f / 448.f);
  rstd = __builtin_amdgcn_rsqf(fmaxf(xhalf_sum(t[1]) * (1.f / 448.f) - mu * mu, 0.f) + EPS);
}
__device__ __forceinline__ unsigned relu2(float a, float b) {
  return __builtin_bit_cast(unsigned, __builtin_elementwise_max(__builtin_bit_cast(s16x2, pk2(a, b)), (s16x2){0, 0}));
}
constexpr int w1_slot(int st) { return st; }
constexpr int w2_pos(int k) { return k < 35 ? k : k - 35; }

struct PairIn { unsigned f; f32x4 v[4]; };
template <int T>
__device__ __forceinline__ void pair_step(const Ctx& c, const f32x16& out, PairIn& in, int ev) {
  if constexpr (T >= 1 && T <= 4)
    *(LAS f32x4*)(c.lds + L_XB + c.w * 4096 + c.lane * 16 + (T - 1) * 1024) = __builtin_shufflevector(out, out, 4 * T - 4, 4 * T - 3, 4 * T - 2, 4 * T - 1);
  if constexpr (T == 4) ev_set(c, ev, c.w);
  if constexpr (T == 7) in.f = *(volatile LAS unsigned*)(c.lds + L_CNT + ev * 16 + (c.w ^ 2) * 4);
  if constexpr (T == 7 || T == 8) {
#pragma unroll
    for (int g = 2 * T - 14; g < 2 * T - 12; ++g) in.v[g] = *(volatile LAS f32x4*)(c.lds + L_XB + (c.w ^ 2) * 4096 + c.lane * 16 + g * 1024);
  }
}
__device__ __forceinline__ void pair_finish(const Ctx& c, f32x16& acc, PairIn& in, int ev) {
  PIN(acc);
  const unsigned fa = c.lbase + L_CNT + ev * 16 + (c.w ^ 2) * 4, sa = c.lbase + L_XB + (c.w ^ 2) * 4096 + c.lane * 16;
#pragma unroll 1
  for (int spins = 0; spins < (1 << 24) && !__builtin_amdgcn_readfirstlane(in.f); ++spins)
    asm volatile("ds_read_b32 %0, %5\n\tds_read_b128 %1, %6\n\tds_read_b128 %2, %6 offset:1024\n\tds_read_b128 %3, %6 offset:2048\n\t"
                 "ds_read_b128 %4, %6 offset:3072\n\ts_waitcnt lgkmcnt(0)"
                 : "=&v"(in.f), "=&v"(in.v[0]), "=&v"(in.v[1]), "=&v"(in.v[2]), "=&v"(in.v[3]) : "v"(fa), "v"(sa) : "memory");
#pragma unroll
  for (int e = 0; e < 16; ++e) acc[e] += in.v[e >> 2][e & 3];
}
constexpr int ms_k(int t, int N, int L = 10) { return t < 2 * N - 2 * L ? t / 2 : (t < 2 * N - L ? t - N + L : t - N); }
constexpr int ms_i(int t, int N, int L = 10) { return t < 2 * N - 2 * L ? 1 - (t & 1) : (t < 2 * N - L ? 1 : 0); }

__device__ __forceinline__ void p345_compute(const Ctx& c, const char* x, char* out, int rb3, int cb3, int rb4, int cb4) {
  const int lane = c.lane, w = c.w, r = c.r, h = c.h;
  lptr lds = c.lds;
  const rsrc_t rs = c.rws;
  const int wr = w & 1, kh = w >> 1, q = (lane & 15) >> 2;
  const int t3 = rb3 * 2 + wr, sub = cb3 * 2 + kh;
  const unsigned trl = ((lane >> 4) & 1) * 32 + (lane & 3) * 8, va = lane * 16;
  const unsigned grp = blockIdx.x & 7, ba = base_a(c, grp), bb_ = base_b(c, grp);
  u32x4 hres[2];
  {
    u32x4 bw[16][2];
    u32x2 xr[4];
    ev_wait<4>(c, EV_WO);
    const unsigned b0 = L_WO + (kh * 224 + 4 * h + q) * 128 + trl;
    unsigned bb[2] = {b0 + ((kh ^ (q >> 1)) * 64), b0 + ((kh ^ 1 ^ (q >> 1)) * 64)};
    PIN(bb[0]); PIN(bb[1]);
#pragma unroll
    for (int ks = 0; ks < 16; ++ks) {
      if (ks == 4) {
        SBAR(); p2_publish(c); STAMP(1, 6);
        const rsrc_t rx = mkrs(x, S_ * D_ * 2);
        const unsigned vx = (unsigned)(t3 * 32 + r) * 896u + h * 8 + sub * 64u;
#pragma unroll
        for (int g = 0; g < 4; ++g) asm volatile("buffer_load_dwordx2 %0, %1, %2, 0 offen offset:%3" : "=v"(xr[g]) : "v"(vx), "s"(rx), "i"(g * 16) : "memory");
      }
#pragma unroll
      for (int i = 0; i < 2; ++i) bw[ks][i] = ldstr2(lds + bb[i] + ((ks >> 1) * 28 + (ks & 1) * 16) * 128, 1024);
    }
    ev_set(c, EV_BPRE, w);
    ev_wait<4>(c, EV_BPRE);
    acquire(c, flag_off(c, 1, (rb3 >> 1) * 16, grp) + (lane & 15) * 256, EV_GO3);
    STAMP(2, 0);
    const unsigned sa = bb_ + 2u * (POS_O + (unsigned)((t3 & 7) * 32 + kh * 16) * 1024u);
    auto so = [&](int g) { return sa + (g & 1) * 4096u + (g >> 1) * 16384u; };
    u32x4 a[16];
    static_for<0, 10>([&](auto kc) { constexpr int k = decltype(kc)::value; ld16<(k & 3) * 1024>(a[k], rs, va, so(k >> 2)); });
    asm volatile("s_waitcnt vmcnt(10)" : "+v"(xr[0]), "+v"(xr[1]), "+v"(xr[2]), "+v"(xr[3]) :: "memory");
    f32x16 acc[2];
#pragma unroll
    for (int g = 0; g < 4; ++g)
#pragma unroll
      for (int i = 0; i < 2; ++i) { acc[0][4 * g + 2 * i] = bflo(xr[g][i]); acc[0][4 * g + 2 * i + 1] = bfhi(xr[g][i]); }
    acc[1] = f32x16{};
    pin16(acc[0]); pin16(acc[1]);
    PairIn in;
    static_for<0, 32>([&](auto tc) {
      constexpr int t = decltype(tc)::value, k = ms_k(t, 16, 5), i = ms_i(t, 16, 5);
      if constexpr (i == 1) waitv<(k <= 6 ? 9 : 15 - k)>(a[k]);
      acc[i] = MFMA(frag(bw[k][i]), frag(a[k]), acc[i]);
      SBAR();
      if constexpr (i == 1 && k + 10 < 16) ld16<((k + 10) & 3) * 1024>(a[k + 10], rs, va, so((k + 10) >> 2));
      pair_step<2 * (t - 27) - 1>(c, acc[1], in, EV_RED3); pair_step<2 * (t - 27)>(c, acc[1], in, EV_RED3);
      SBAR();
    });
    STAMP(2, 7);
    pair_finish(c, acc[0], in, EV_RED3);
    STAMP(2, 8);
#pragma unroll
    for (int i = 0; i < 8; ++i) hres[i >> 2][i & 3] = pk2(acc[0][2 * i], acc[0][2 * i + 1]);
#pragma unroll
    for (int sx = 0; sx < 2; ++sx) st16s(sx, hres[sx], rs, va, bb_ + spread(POS_H + (unsigned)((t3 & 7) * 32 + sub * 2) * 1024u));
    f32x4 s4 = {}, q4 = {};
#pragma unroll
    for (int g = 0; g < 4; ++g) {
      const f32x4 v = {acc[0][4 * g], acc[0][4 * g + 1], acc[0][4 * g + 2], acc[0][4 * g + 3]};
      s4 += v; q4 += v * v;
    }
    const float s = xhalf_sum((s4[0] + s4[1]) + (s4[2] + s4[3])), sq = xhalf_sum((q4[0] + q4[1]) + (q4[2] + q4[3]));
    if (h == 0) __builtin_amdgcn_raw_buffer_store_b64((u32x2){__float_as_uint(s), __float_as_uint(sq)}, rs, r * 8, ba + spread(POS_ST + (t3 & 7) * 4096u) + sub * 256u, 16);
    STAMP(2, 9);
  }
  {
    const int t4 = rb4 * 4 + w;
    unsigned bo[4][3];
#pragma unroll
    for (int i = 0; i < 4; ++i) {
      bo[i][0] = (4 * h + q) * 256 + ((i ^ q) * 64) + trl; bo[i][1] = bo[i][0] + 65536; bo[i][2] = bo[i][0] + 131072;
      PIN(bo[i][0]); PIN(bo[i][1]); PIN(bo[i][2]);
    }
    publish<4>(c, EV_PUB3, flag_off(c, 2, rb3 * 8 + cb3, grp));
    STAMP(2, 10);
    ev_wait<4>(c, EV_CS);
    u32x2 cf[4];
#pragma unroll
    for (int i = 0; i < 4; ++i) { const u32x4 t = colfrag(*(LAS float*)(lds + L_CS + i * 128 + r * 4), h); cf[i] = (u32x2){t[0], t[1]}; PIN(cf[i]); }
    u32x4 a[28], bq[4][2];
    auto bread = [&](auto kc) {
      constexpr int k = decltype(kc)::value, pass = k / 28, ks = k % 28, off = w1_slot(ks >> 2) * SLOT + (ks & 3) * 4096;
      bq[k & 3][0] = ldstr2(lds + bo[2 * pass][off >> 16] + (off & 65535), 2048);
      bq[k & 3][1] = ldstr2(lds + bo[2 * pass + 1][off >> 16] + (off & 65535), 2048);
    };
    bread(IC<0>{}); bread(IC<1>{}); bread(IC<2>{});
    SBAR();
    const int rot = p4_rot(cb4);
    unsigned sa4[7];
#pragma unroll
    for (int g = 0; g < 7; ++g) { sa4[g] = bb_ + 2u * (POS_H + (t4 & 7) * 32768u) + spread((g + rot >= 7 ? g + rot - 7 : g + rot) * 4096u); asm volatile("" : "+s"(sa4[g])); }
    const unsigned sg = ba + 2u * (POS_G + (unsigned)((t4 & 7) * 112 + cb4 * 8) * 1024u);
    { const int l = lane < 14 ? lane : 0; acquire(c, flag_off(c, 2, rb4 * 16, grp) + (l >= 7 ? l + 1 : l) * 256, EV_GO4); }
    STAMP(3, 0);
    auto sa = [&](int g) { return sa4[g]; };
    f32x2 sv[7];
    static_for<0, 12>([&](auto kc) { constexpr int k = decltype(kc)::value; ld16<(k & 3) * 1024>(a[k], rs, va, sa(k >> 2)); });
    f32x16 acc[4] = {};
    u32x4 og, rf;
    static_for<0, 56>([&](auto kc) {
      constexpr int k = decltype(kc)::value, pass = k / 28, ks = k % 28;
      if constexpr (k < 28) waitv<((k + 11 < 27 ? k + 11 : 27) - k + (k >= 9 && k <= 20 ? 7 : 0))>(a[k]);
      if constexpr (k == 28) {
#pragma unroll
        for (int i = 0; i < 4; ++i) acc[i] = MFMA(frag((u32x4){cf[i][0], cf[i][1], 0u, 0u}), frag(rf), acc[i]);
        SBAR();
      }
      acc[2 * pass] = MFMA(frag(bq[k & 3][0]), frag(a[ks]), acc[2 * pass]);
      SBAR();
      if constexpr (k + 12 < 28) ld16<((k + 12) & 3) * 1024>(a[k + 12], rs, va, sa((k + 12) >> 2));
      if constexpr (k == 8) stats_issue(c, t4, sv);
      if constexpr (k + 3 < 56) bread(IC<k + 3>{});
      SBAR();
      acc[2 * pass + 1] = MFMA(frag(bq[k & 3][1]), frag(a[ks]), acc[2 * pass + 1]);
      SBAR();
      if constexpr (k == 21) {
        stats_pin(sv);
        float t = sv[0][0];
#pragma unroll
        for (int i = 1; i < 7; ++i) { t += sv[i][0]; pinf(t); }
        rf = rowfrag(xhalf_sum(t) * (-1.f / 448.f));
        pin4(rf);
        SBAR();
      }
      if constexpr (k == 24) { ev_set(c, EV_A4, w); SBAR(); }
      if constexpr (k >= 30 && k < 46) {
        constexpr int e = k - 30;
        og[e & 3] = relu2(acc[e >> 3][(e & 7) * 2], acc[e >> 3][(e & 7) * 2 + 1]);
        PIN(og);
        if constexpr ((e & 3) == 3) st16<(e >> 2) * 1024>(og, rs, va, sg);
        SBAR();
      }
    });
    ev_set(c, EV_X4, w);
    STAMP(3, 8);
    static_for<0, 4>([&](auto fc) {
      constexpr int f = decltype(fc)::value;
      u32x4 o;
#pragma unroll
      for (int e = 0; e < 4; ++e) o[e] = relu2(acc[2 + (f >> 1)][8 * (f & 1) + 2 * e], acc[2 + (f >> 1)][8 * (f & 1) + 2 * e + 1]);
      st16<f * 1024>(o, rs, va, sg + 4096u);
    });
    STAMP(3, 9);
    publish<4>(c, EV_PUB4, flag_off(c, 3, rb4 * 16 + cb4, grp));
    STAMP(3, 10);
  }
  {
    unsigned bo[2][3];
#pragma unroll
    for (int i = 0; i < 2; ++i) {
      bo[i][0] = kh * 2048 + (4 * h + q) * 128 + ((kh ^ i ^ (q >> 1)) * 64) + trl; bo[i][1] = bo[i][0] + 65536; bo[i][2] = bo[i][0] + 131072;
      PIN(bo[i][0]); PIN(bo[i][1]); PIN(bo[i][2]);
    }
    const int rot = p5_rot(cb3);
    const unsigned s5a = ba + 2u * (POS_G + (unsigned)((t3 & 7) * 112 + kh * 56 + rot * 4) * 1024u), s5b = rot ? s5a - 2u * 57344u : s5a;
    acquire(c, flag_off(c, 3, (rb3 >> 1) * 16, grp) + (lane < 14 ? lane : 0) * 256, EV_GO5);
    STAMP(4, 0);
    auto sa = [&](int g) { return (g >= 8 ? s5b : s5a) + spread(g * 4096u); };
    u32x4 a[W5], bq[8];
    f32x2 sv[7];
    static_for<0, W5>([&](auto kc) { constexpr int k = decltype(kc)::value; ld16<(k & 3) * 1024>(a[k], rs, va, sa(k >> 2)); });
    auto bread = [&](auto tc) {
      constexpr int t = decltype(tc)::value, off = w2_pos(ms_k(t, 56)) * 4096;
      bq[t & 7] = ldstr2(lds + bo[ms_i(t, 56)][off >> 16] + (off & 65535), 1024);
    };
    ev_wait_half(c, EV_W2A);
    static_for<0, 6>(bread);
    f32x16 acc[2] = {};
    float rstd;
    PairIn in;
    static_for<0, 112>([&](auto tc) {
      constexpr int t = decltype(tc)::value, k = ms_k(t, 56), i = ms_i(t, 56);
      if constexpr (i == 1) waitv<((k + W5 - 1 < 55 ? k + W5 - 1 : 55) - k + (k > 26 && k <= 26 + W5 ? 7 : 0))>(a[k % W5]);
      acc[i] = MFMA(frag(bq[t & 7]), frag(a[k % W5]), acc[i]);
      SBAR();
      if constexpr (t == 8 || t == 22 || t == 36) ev_set(c, EV_SEG0 + (t - 8) / 14, w);
      if constexpr (t == 64) { pin16(acc[0]); pin16(acc[1]); ev_wait_half(c, EV_W2B); }
      if constexpr (t + 6 < 112) bread(IC<t + 6>{});
      if constexpr (i == 0 && k + W5 < 56) ld16<((k + W5) & 3) * 1024>(a[k % W5], rs, va, sa((k + W5) >> 2));
      if constexpr (t == 53) stats_issue(c, t3, sv);
      if constexpr (t == 86) {
        stats_pin(sv);
        float t0 = sv[0][0], t1 = sv[0][1];
#pragma unroll
        for (int e = 1; e < 7; ++e) { t0 += sv[e][0]; t1 += sv[e][1]; pinf(t0); pinf(t1); }
        sv[0] = (f32x2){t0, t1};
      }
      if constexpr (t == 90) {
        const float mu = xhalf_sum(sv[0][0]) * (1.f / 448.f);
        rstd = __builtin_amdgcn_rsqf(fmaxf(xhalf_sum(sv[0][1]) * (1.f / 448.f) - mu * mu, 0.f) + EPS);
        pinf(rstd);
      }
      pair_step<t - 102>(c, acc[1], in, EV_RED5);
      SBAR();
    });
    STAMP(4, 15);
    pair_finish(c, acc[0], in, EV_RED5);
    STAMP(4, 16);
#pragma unroll
    for (int sx = 0; sx < 2; ++sx) {
      u32x4 o;
#pragma unroll
      for (int i = 0; i < 4; ++i) o[i] = pk2(acc[0][8 * sx + 2 * i] * rstd + bflo(hres[sx][i]), acc[0][8 * sx + 2 * i + 1] * rstd + bfhi(hres[sx][i]));
      auto x0 = __builtin_amdgcn_permlane32_swap(o[0], o[2], false, false);
      auto x1 = __builtin_amdgcn_permlane32_swap(o[1], o[3], false, false);
      *(u32x4*)(out + (unsigned)(t3 * 32 + r) * 896u + (sub * 32 + 16 * sx + 8 * h) * 2) = (u32x4){x0[0], x1[0], x0[1], x1[1]};
    }
    STAMP(4, 18);
  }
}

__device__ __forceinline__ void probe(const Ctx& c, int id, unsigned off) {
  unsigned best[2] = {~0u, ~0u};
#pragma unroll 1
  for (int i = 0; i < 6; ++i) {
    unsigned long long t0, t1;
    const unsigned z = 0;
    asm volatile("s_memtime %0\n\ts_waitcnt lgkmcnt(0)\n\tbuffer_store_dword %2, off, %3, %4 sc1\n\ts_waitcnt vmcnt(0)\n\ts_memtime %1\n\ts_waitcnt lgkmcnt(0)"
                 : "=&s"(t0), "=&s"(t1) : "v"(z), "s"(c.rws), "s"(off + (i & 1) * 8192u) : "memory");
    const unsigned d = (unsigned)(t1 - t0);
    if (i & 1) best[1] = min(best[1], d); else best[0] = min(best[0], d);
  }
  if (c.lane == 0) __builtin_amdgcn_raw_buffer_store_b64((u32x2){best[1] < best[0] ? 1u : 0u, c.tag}, c.rws, id * 8, OFF_PR, 16);
}
__device__ __forceinline__ void fetch_map(const Ctx& c) {
  const unsigned vo = (c.lane & 15) * 8;
  u32x2 g;
#pragma unroll 1
  for (int spins = 0; spins < (1 << 17); ++spins) {
    asm volatile("buffer_load_dwordx2 %0, %1, %2, %3 offen sc1\n\ts_waitcnt vmcnt(0)" : "=v"(g) : "v"(vo), "s"(c.rws), "s"(OFF_PR) : "memory");
    if (__all(g[1] == c.tag)) break;
    __builtin_amdgcn_s_sleep(8);
  }
  ev_set(c, EV_MAP, 0, ((unsigned)__ballot(g[0] & 1) & 0xFFFFu) | 0x80000000u);
}

__device__ __forceinline__ void helper(const Ctx& c, const char* wo, const char* w1, const char* w2, int cb3, int cb4) {
  const int lane = c.lane, w = c.w;
  const rsrc_t rwo = mkrs(wo, D_ * D_ * 2), rw1 = mkrs(w1, D_ * DFF * 2), rw2 = mkrs(w2, D_ * DFF * 2);
  const unsigned vw1 = (unsigned)(lane >> 4) * 3584u + (unsigned)((lane & 15) ^ ((lane >> 4) << 2)) * 16u;
  const unsigned vw2 = (unsigned)(lane >> 3) * 896u + (unsigned)((lane & 7) ^ (((lane >> 4) & 1) << 2)) * 16u;
  const int rot5 = p5_rot(cb3) * 4;
  const unsigned sw0 = (unsigned)((w >> 1) * 896 + (w & 1) * 8 + rot5 * 16) * 896u + cb3 * 128u;
  auto sw = [&](int k) { return sw0 + k * 14336u - (k + rot5 >= 56 ? 56u * 14336u : 0u); };
  u32x4 s1[16], st[49];
  auto ldw = [&](auto ic) { constexpr int i = decltype(ic)::value, k = i < 28 ? i : i + 7; ld16p(st[i], rw2, vw2, sw(k)); };
  __builtin_amdgcn_s_sleep(HDELAY);
#pragma unroll 1
  for (int p = w; p < 57; p += 4) { dma16(rwo, vw2, p * 7168u + cb3 * 128u, c.lbase + L_WO + p * 1024); pace(); }
#pragma unroll 1
  for (int p = w; p < 48; p += 4) { dma16(rw1, vw1, w1_piece(p, cb4), c.lbase + p * 1024); pace(); }
  static_for<0, 16>([&](auto ic) { constexpr int i = decltype(ic)::value; ld16p(s1[i], rw1, vw1, w1_piece(48 + i * 4 + w, cb4)); pace(); });
  static_for<0, NW2>([&](auto ic) {
    ldw(ic);
    pace();
    if constexpr (decltype(ic)::value == 15) { waitv<16>(s1[15]); ev_set(c, EV_WO, w); if (w == 3) fetch_map(c); }
  });
  STAMP(1, 16);
  unsigned wb[3] = {(unsigned)(w * 1024 + lane * 16), 0u, 0u};
  wb[1] = wb[0] + 65536; wb[2] = wb[0] + 131072;
  PIN(wb[0]); PIN(wb[1]); PIN(wb[2]);
  ev_wait<4, 1>(c, EV_BPRE);
  static_for<0, 16>([&](auto ic) {
    constexpr int i = decltype(ic)::value, off = 3 * SLOT + i * 4096;
    waitv<NW2>(s1[i]);
    *(LAS u32x4*)(c.lds + wb[off >> 16] + (off & 65535)) = s1[i];
  });
  ev_set(c, EV_W1, w);
  ev_wait<4>(c, EV_W1);
  {
    const int q = (lane & 15) >> 2;
    lptr bp = c.lds + (4 * c.h + q) * 256 + ((w ^ q) * 64) + ((lane >> 4) & 1) * 32 + (lane & 3) * 8;
    float cs[4] = {0.f, 0.f, 0.f, 0.f};
#pragma unroll 1
    for (int sl = 0; sl < 2; ++sl) {
      u32x2 b[28];
#pragma unroll
      for (int k = 0; k < 28; ++k) b[k] = ldstr(bp + sl * 57344 + k * 2048);
      SBAR();
#pragma unroll
      for (int k = 0; k < 28; ++k) { cs[(k & 1) * 2] = dot2(b[k][0], ONES2, cs[(k & 1) * 2]); cs[(k & 1) * 2 + 1] = dot2(b[k][1], ONES2, cs[(k & 1) * 2 + 1]); }
    }
    *(LAS float*)(c.lds + L_CS + w * 128 + c.r * 4) = xhalf_sum((cs[0] + cs[1]) + (cs[2] + cs[3]));
    ev_set(c, EV_CS, w);
  }
  ev_wait<4, 2>(c, EV_A4);
#pragma unroll 1
  for (int k = 28; k < 35; ++k) dma16(rw2, vw2, sw(k), c.lbase + k * 4096 + w * 1024);
  static_for<NW2, 49>(ldw);
  auto stw = [&](auto ic) {
    constexpr int i = decltype(ic)::value, k = i < 28 ? i : i + 7, off = w2_pos(k) * 4096;
    waitv<0>(st[i]);
    *(LAS u32x4*)(c.lds + wb[off >> 16] + (off & 65535)) = st[i];
  };
  ev_wait<4, 2>(c, EV_X4);
  static_for<0, 28>(stw);
  ev_set(c, EV_W2A, w);
  ev_wait_half<2>(c, EV_SEG0);
  static_for<28, 35>(stw);
  ev_wait_half<2>(c, EV_SEG1);
  static_for<35, 42>(stw);
  ev_wait_half<2>(c, EV_SEG2);
  static_for<42, 49>(stw);
  ev_set(c, EV_W2B, w);
}

__global__ __launch_bounds__(512, 2) void block_fwd(const char* __restrict__ x, const char* __restrict__ wqkv, const char* __restrict__ wo,
                                                    const char* __restrict__ w1, const char* __restrict__ w2,
                                                    char* __restrict__ ws, char* __restrict__ out) {
  extern __shared__ __attribute__((aligned(16))) char smem[];
  Ctx c;
  c.lds = (lptr)smem; c.lbase = (unsigned)(uintptr_t)smem;
  c.lane = threadIdx.x & 63; c.w8 = __builtin_amdgcn_readfirstlane(threadIdx.x >> 6); c.w = c.w8 & 3;
  c.r = c.lane & 31; c.h = c.lane >> 5;
  const int xcd = blockIdx.x & 7, j = blockIdx.x >> 3;
  {
    unsigned long long pc;
    asm volatile("s_getpc_b64 %0" : "=s"(pc));
    touch_lds(c, mkrs((const void*)(pc & ~255ull), CODE_BYTES), c.lane < 2 ? c.lane * 128u : OOB, (c.w8 * 32 + j) * 256u);
  }
  asm volatile("" :: "s"(x), "s"(wqkv), "s"(wo), "s"(w1), "s"(w2), "s"(ws), "s"(out));
  c.ws = ws;
  c.wb = (char*)(((uintptr_t)ws + 0x1FFFFFull) & ~0x1FFFFFull);
  c.map = 0; c.home = 0;
  c.rws = mkrs(c.wb, WS_USED);
  const int rb1 = (xcd & 3) * 4 + (j & 3), cb1 = ((xcd < 4 ? 0xCB765210u : 0xFEDA9843u) >> ((j >> 2) * 4)) & 15;
  const int rb3 = xcd * 4 + (j & 3), cb3 = j >> 2, rb4 = xcd * 2 + (j & 1), cb4 = j >> 1;
  c.tag = 0;
  if (c.w8 >= 4) {
    STAMP(0, 0);
    p1_load(c, x, wqkv, rb1, cb1);
  } else {
    p1_stats(c, x, wqkv, wo, w1, w2, out, rb1, cb1);
  }
  int role = c.w8;
  asm volatile("" : "+s"(role));
  if (role >= 4) {
    ev_wait<4, 1>(c, EV_P1D);
    ev_wait<1, 2>(c, EV_GO2);
    __builtin_amdgcn_s_sleep(ADELAY);
    if (c.w >= 1) __builtin_amdgcn_s_sleep(STAG);
    if (c.w >= 2) __builtin_amdgcn_s_sleep(2 * STAG);
    if (c.w < 3) touch_lds(c, mkrs(c.w == 0 ? wo : (c.w == 1 ? w1 : w2), c.w ? D_ * DFF * 2 : D_ * D_ * 2), c.lane < (c.w ? 49 : 13) ? c.lane * 128u : OOB,
                           blockIdx.x * (c.w ? 6272u : 1568u));
    if (c.w < 1) __builtin_amdgcn_s_sleep(STAG);
    if (c.w < 2) __builtin_amdgcn_s_sleep(2 * STAG);
    if (j < 28) helper(c, wo, w1, w2, cb3, cb4);
    else {
      if (c.w == 3 && j == 28) probe(c, xcd, OFF_A + (xcd << 21) + 0x1F0000u);
      if (c.w == 3 && j == 29) probe(c, 8 + xcd, OFF_B + (xcd << 20) + spread(POS_H + 28672u));
      if (c.w == 0) { __builtin_amdgcn_s_sleep(100); fetch_map(c); }
    }
    return;
  }
  if (cb1 < 10) p1_compute<3>(c, rb1, cb1);
  else if (cb1 == 10) p1_compute<2>(c, rb1, cb1);
  else p1_compute<0>(c, rb1, cb1);
  {
    const unsigned addr = c.lbase + L_CNT + EV_PUB1 * 16, one = 1, val = c.tag | c.home, vo = c.lane * 8192u;
    unsigned old;
    asm volatile("s_waitcnt vmcnt(0)\n\ts_mov_b64 exec, 1\n\tds_add_rtn_u32 %0, %1, %2\n\ts_mov_b64 exec, -1\n\ts_waitcnt lgkmcnt(0)"
                 : "=v"(old) : "v"(addr), "v"(one) : "memory");
    if (__builtin_amdgcn_readfirstlane(old) == 3)
      asm volatile("s_mov_b64 exec, 3\n\tbuffer_store_dword %0, %1, %2, %3 offen sc1\n\ts_mov_b64 exec, -1"
                   :: "v"(val), "v"(vo), "s"(c.rws), "s"(flag1_off(cb1 * 16 + rb1)) : "memory");
  }
  ev_set(c, EV_P1D, c.w);
  STAMP(0, 10);
  p2_attn(c);
  if (j >= 28) {
    p2_publish(c);
    if (blockIdx.x == 255 && c.w8 == 0) {
#pragma unroll 1
      for (int i = 0; i < 4; ++i) poll(c, flag1_off(i * 64 + c.lane));
      if (c.lane == 0) __builtin_amdgcn_raw_buffer_store_b32(c.tag, c.rws, 0, OFF_EP, 16);
    }
    return;
  }
  p345_compute(c, x, out, rb3, cb3, rb4, cb4);
}

extern "C" void kernel_launch(void* const* d_in, const int* in_sizes, int n_in, void* d_out, int out_size,
                              void* d_ws, size_t ws_size, hipStream_t stream) {
  (void)hipFuncSetAttribute((const void*)block_fwd, hipFuncAttributeMaxDynamicSharedMemorySize, LDS_BYTES);
  block_fwd<<<256, 512, LDS_BYTES, stream>>>((const char*)d_in[0], (const char*)d_in[1], (const char*)d_in[2],
                                             (const char*)d_in[4], (const char*)d_in[5], (char*)d_ws, (char*)d_out);
}
```
